```python
import torch

def _softmax(x, axis=-1):
    return torch.softmax(x, dim=axis)
def _gelu(x):
    return torch.nn.functional.gelu(x, approximate="tanh")
def _silu(x):
    return torch.nn.functional.silu(x)
def _sigmoid(x):
    return torch.sigmoid(x)
def _layernorm(x, axis=-1, eps=1e-5):
    m = x.mean(dim=axis, keepdim=True)
    v = x.var(dim=axis, keepdim=True, unbiased=False)
    return (x - m) / torch.sqrt(v + eps)
def _scatter_add(tgt, idx, src, axis=0):
    return tgt.clone().index_add_(axis, idx, src)
def _segment_sum(src, seg, n):
    out = torch.zeros((n,) + src.shape[1:], dtype=src.dtype)
    return out.index_add_(0, seg, src)
def _pad(x, pw):
    flat = tuple(v for pair in reversed(pw) for v in pair)
    return torch.nn.functional.pad(x, flat)

kernel_name = 'interleave_layers__ssm_conv_h3__dense_activation_mish__prompt'

def setup_inputs():

    _w = (lambda a: (lambda t: t.to(torch.bfloat16) if t.is_floating_point() else t)(torch.as_tensor(a)))
    def _A_setup():
        B, S, D = 1, 2048, 640
        ks = 4
        return {
            "q": _w((lambda *s: torch.randn(*s, dtype=torch.bfloat16) * 0.05)(B, S, D)),
            "k": _w((lambda *s: torch.randn(*s, dtype=torch.bfloat16) * 0.2)(B, S, D)),
            "v": _w((lambda *s: torch.randn(*s, dtype=torch.bfloat16) * 0.2)(B, S, D)),
            "shift_k": _w((lambda *s: torch.randn(*s, dtype=torch.bfloat16) * 0.2)(D, ks) * 0.5),
            "ssm_k": _w((lambda *s: torch.randn(*s, dtype=torch.bfloat16) * 0.4)(D, S) * (1.0 / S ** 0.5)),
            "ssm_D": _w((lambda *s: torch.randn(*s, dtype=torch.bfloat16) * 0.2)(D)),
        }
    _A_in = _A_setup()

    def _B_setup():
        B, S, D, F = 1, 2048, 640, 1728
        return {
            "x": _w((lambda *s: torch.randn(*s, dtype=torch.bfloat16) * 0.05)(B, S, D)),
            "w1": _w((lambda *s: torch.randn(*s, dtype=torch.bfloat16) * 0.8)(D, F) * (D ** -0.5)),
            "b1": _w((lambda *s: torch.randn(*s, dtype=torch.bfloat16) * 0.2)(F)),
            "w2": _w((lambda *s: torch.randn(*s, dtype=torch.bfloat16) * 0.1)(F, D) * (F ** -0.5)),
        }
    _B_in = _B_setup()

    return {
        "A_q": _A_in["q"],
        "A_k": _A_in["k"],
        "A_v": _A_in["v"],
        "A_shift_k": _A_in["shift_k"],
        "A_ssm_k": _A_in["ssm_k"],
        "A_ssm_D": _A_in["ssm_D"],
        "B_x": _B_in["x"],
        "B_w1": _B_in["w1"],
        "B_b1": _B_in["b1"],
        "B_w2": _B_in["w2"],
    }

def reference(A_q, A_k, A_v, A_shift_k, A_ssm_k, A_ssm_D, B_x, B_w1, B_b1, B_w2):
    def _A_ref(q, k, v, shift_k, ssm_k, ssm_D):
        B, S, D = q.shape
        ks = shift_k.shape[-1]
        k_pad = _pad(k, ((0, 0), (ks - 1, 0), (0, 0)))
        k_shift = torch.zeros_like(k)
        for t in range(ks):
            k_shift = k_shift + k_pad[:, t:t + S, :] * shift_k[:, ks - 1 - t][None, None, :]
        kv = k_shift * v
        L = 2 * S
        kv_pad = _pad(kv, ((0, 0), (0, S), (0, 0)))
        kf_pad = _pad(ssm_k, ((0, 0), (0, S)))
        kv_f = (lambda x, axes=(-2,-1): torch.fft.rfft2(x.float(), dim=axes))(kv_pad[:, None, :, :], axes=(1, 2))
        kf_f = (lambda x, axes=(-2,-1): torch.fft.rfft2(x.float(), dim=axes))(kf_pad[:, None, :], axes=(1, 2))
        kf_f = (lambda x, *d: x.permute(*d))(kf_f, 1, 2, 0)
        y_f = kv_f * kf_f[None, :, :, :]
        y = (lambda x, s, axes=(-2,-1): torch.fft.irfft2(x, s=s, dim=axes).to(torch.bfloat16))(y_f, (1, L), axes=(1, 2))[:, 0, :S, :]
        y = y + kv * ssm_D[None, None, :]
        return q * y

    def _B_ref(x, w1, b1, w2):
        h = torch.einsum("bsd,df->bsf", x, w1) + b1
        sp = torch.log1p(torch.exp(-torch.abs(h))) + torch.where(h > 0, h, torch.zeros_like(h))
        act = h * torch.tanh(sp)
        return torch.einsum("bsf,fd->bsd", act, w2)

    x = A_q
    x = x + _A_ref(_layernorm(x, axis=-1), A_k, A_v, A_shift_k, A_ssm_k, A_ssm_D)
    x = x + _B_ref(_layernorm(x, axis=-1), B_w1, B_b1, B_w2)
    x = x + _A_ref(_layernorm(x, axis=-1), A_k, A_v, A_shift_k, A_ssm_k, A_ssm_D)
    x = x + _B_ref(_layernorm(x, axis=-1), B_w1, B_b1, B_w2)
    return x
```

```cpp
#include <hip/hip_runtime.h>
#include <hip/hip_bf16.h>
#include <stdint.h>
typedef unsigned short u16;
constexpr int S=2048, D=640, F=1728;
__device__ inline float bf2f(u16 x){ return __uint_as_float(((unsigned)x)<<16); }
__device__ inline u16 f2bf(float f){ unsigned u=__float_as_uint(f); u += 0x7fffu + ((u>>16)&1u); return (u16)(u>>16); }
__device__ inline float rb(float f){ return bf2f(f2bf(f)); }

__global__ void k_kv(const u16* k, const u16* v, const u16* sh, u16* kv){
  int i = blockIdx.x*256+threadIdx.x; if(i>=S*D) return;
  int s=i/D, d=i%D; float acc=0.f;
  for(int t=0;t<4;t++){ int ss=s-3+t; float kk = ss>=0? bf2f(k[ss*D+d]):0.f; acc += kk*bf2f(sh[d*4+3-t]); }
  kv[i]=f2bf(acc*bf2f(v[i]));
}
__global__ void k_conv(const u16* kv, const u16* h, const u16* Dv, u16* y){
  int i = blockIdx.x*256+threadIdx.x; if(i>=S*D) return;
  int s=i/D, d=i%D; float acc=0.f;
  for(int j=0;j<=s;j++) acc += bf2f(kv[j*D+d])*bf2f(h[d*S+s-j]);
  y[i]=f2bf(acc + bf2f(kv[i])*bf2f(Dv[d]));
}
__device__ inline float wsum(float x){ for(int o=32;o>0;o>>=1) x+=__shfl_xor(x,o); return x; }
__global__ void k_ln(const u16* xin, const u16* y, u16* xo, u16* a){
  int s=blockIdx.x, l=threadIdx.x; float x[10]; float sm=0;
  for(int j=0;j<10;j++){ x[j]=bf2f(xin[s*D+l+64*j]); sm+=x[j]; }
  float m=wsum(sm)/D, vv=0; for(int j=0;j<10;j++) vv+=(x[j]-m)*(x[j]-m);
  float r=rsqrtf(wsum(vv)/D+1e-5f); sm=0;
  for(int j=0;j<10;j++){ x[j]=rb(x[j]+(x[j]-m)*r*bf2f(y[s*D+l+64*j])); sm+=x[j]; xo[s*D+l+64*j]=f2bf(x[j]); }
  m=wsum(sm)/D; vv=0; for(int j=0;j<10;j++) vv+=(x[j]-m)*(x[j]-m);
  r=rsqrtf(wsum(vv)/D+1e-5f);
  for(int j=0;j<10;j++) a[s*D+l+64*j]=f2bf((x[j]-m)*r);
}
__global__ void k_g1(const u16* a, const u16* w1, const u16* b1, u16* H){
  int i = blockIdx.x*256+threadIdx.x; if(i>=S*F) return;
  int s=i/F, f=i%F; float acc=0.f;
  for(int k=0;k<D;k++) acc += bf2f(a[s*D+k])*bf2f(w1[k*F+f]);
  float h=acc+bf2f(b1[f]);
  float w=__expf(fminf(h,30.f)); float n=w*(w+2.f);
  H[i]=f2bf(h*n/(n+2.f));
}
__global__ void k_g2(const u16* H, const u16* w2, const u16* xin, u16* xo){
  int i = blockIdx.x*256+threadIdx.x; if(i>=S*D) return;
  int s=i/D, d=i%D; float acc=0.f;
  for(int k=0;k<F;k++) acc += bf2f(H[s*F+k])*bf2f(w2[k*D+d]);
  xo[i]=f2bf(bf2f(xin[i])+acc);
}
extern "C" void kernel_launch(void* const* d_in, const int* in_sizes, int n_in, void* d_out, int out_size, void* d_ws, size_t ws_size, hipStream_t stream) {
  const u16 *q=(const u16*)d_in[0], *k=(const u16*)d_in[1], *v=(const u16*)d_in[2], *sh=(const u16*)d_in[3], *h=(const u16*)d_in[4], *Dv=(const u16*)d_in[5];
  const u16 *w1=(const u16*)d_in[7], *b1=(const u16*)d_in[8], *w2=(const u16*)d_in[9];
  u16* ws=(u16*)d_ws; u16 *kv=ws, *y=ws+S*D, *x1=ws+2*S*D, *a=ws+3*S*D, *x2=ws+4*S*D, *H=ws+5*S*D;
  k_kv<<<S*D/256,256,0,stream>>>(k,v,sh,kv);
  k_conv<<<S*D/256,256,0,stream>>>(kv,h,Dv,y);
  k_ln<<<S,64,0,stream>>>(q,y,x1,a);
  k_g1<<<S*F/256,256,0,stream>>>(a,w1,b1,H);
  k_g2<<<S*D/256,256,0,stream>>>(H,w2,x1,x2);
  k_ln<<<S,64,0,stream>>>(x2,y,x1,a);
  k_g1<<<S*F/256,256,0,stream>>>(a,w1,b1,H);
  k_g2<<<S*D/256,256,0,stream>>>(H,w2,x1,(u16*)d_out);
}
```

```cpp
#include <hip/hip_runtime.h>
#include <stdint.h>

#define DI __device__ __forceinline__
#define LAS __attribute__((address_space(3)))
typedef unsigned v4u __attribute__((ext_vector_type(4)));
typedef unsigned v2u __attribute__((ext_vector_type(2)));
typedef float f32x4 __attribute__((ext_vector_type(4)));
typedef float f32x2 __attribute__((ext_vector_type(2)));
typedef short s16x4 __attribute__((ext_vector_type(4)));
typedef __bf16 bf16x2 __attribute__((ext_vector_type(2)));
typedef __amdgpu_buffer_rsrc_t rsrc_t;

constexpr int S = 2048, D = 640, F = 1728;
constexpr int SD2 = S * D * 2;
constexpr int SC1 = 16;
constexpr int OOB = 0x7f000000;
constexpr int O_INIT = 256;
constexpr int O_TMO = 3072;
constexpr int O_CTR = 4096;
constexpr int C_S0U = 0, C_S0W = 8, C_S1 = 72, C_G = 200, C_N = 296;
constexpr int O_ST2 = 131072;
constexpr int O_ST3 = O_ST2 + 524288;
constexpr int O_STAMP = O_ST3 + 262144;
constexpr int O_U = 1048576;
constexpr int O_Q8 = O_U + SD2;
constexpr int O_W1T = O_Q8 + 2 * SD2;
constexpr int O_W2T = O_W1T + D * F * 2;
constexpr int O_X1 = O_W2T + D * F * 2;
constexpr int O_Y = O_X1 + SD2;
constexpr int O_X3 = O_Y + SD2;
constexpr int O_H1 = O_X3 + SD2;
constexpr int O_H2 = O_H1 + S * F * 2;
constexpr int WS_END = O_H2 + S * F * 2;
constexpr int LDS_BYTES = 163840;
constexpr int L_KV = 0, KVS = 176;
constexpr int L_WST = 11264, WSTS = 144;
constexpr int L_Q = L_WST + 4 * 4608;
constexpr int L_CONV = 120320, CONVW = 8768;
constexpr int L_YI = L_CONV + 4 * CONVW, YSTR = 2064;
constexpr int L_RED = 143360;
constexpr int L_CB = L_RED + 15360;
static_assert(L_YI + 4 * YSTR <= LDS_BYTES && L_CB + 896 <= LDS_BYTES, "lds");
constexpr unsigned SPIN_CAP = 1u << 20;

#define STAMPS 0

DI v4u ld16(rsrc_t rs, int vo, int so, int aux) { return aux ? __builtin_amdgcn_raw_buffer_load_b128(rs, vo, so, SC1) : __builtin_amdgcn_raw_buffer_load_b128(rs, vo, so, 0); }
DI v2u ld8(rsrc_t rs, int vo, int so, int aux) { return aux ? __builtin_amdgcn_raw_buffer_load_b64(rs, vo, so, SC1) : __builtin_amdgcn_raw_buffer_load_b64(rs, vo, so, 0); }
DI void st16(v4u v, rsrc_t rs, int vo, int so) {
  asm volatile("" : "+v"(v));
  __builtin_amdgcn_raw_buffer_store_b128(v, rs, vo, so, SC1);
  asm volatile("s_nop 1" :: "v"(v));
}
DI void st8(v2u v, rsrc_t rs, int vo, int so) { __builtin_amdgcn_raw_buffer_store_b64(v, rs, vo, so, SC1); }
DI rsrc_t mk(const void* p, int bytes) { return __builtin_amdgcn_make_buffer_rsrc((void*)p, 0, bytes, 0x00020000); }
DI float blo(unsigned u) { return __uint_as_float(u << 16); }
DI float bhi(unsigned u) { return __uint_as_float(u & 0xffff0000u); }
DI unsigned pk(float a, float b) { f32x2 f = {a, b}; return __builtin_bit_cast(unsigned, __builtin_convertvector(f, bf16x2)); }
DI v2u trd(LAS unsigned char* p) { return __builtin_bit_cast(v2u, __builtin_amdgcn_ds_read_tr16_b64_v4i16((LAS s16x4*)p)); }
DI v4u cat(v2u a, v2u b) { return v4u{a.x, a.y, b.x, b.y}; }
#define LDS16(p) (*(LAS v4u*)(p))
#define LDS8(p) (*(LAS v2u*)(p))
#define LDSF4(p) (*(LAS f32x4*)(p))
#define BAR() asm volatile("s_waitcnt lgkmcnt(0)\n\ts_barrier" ::: "memory")
#define VMDRAIN() asm volatile("s_waitcnt vmcnt(0)" ::: "memory")
#define SB() __builtin_amdgcn_sched_barrier(0)
DI void mf_vv(f32x4& c, v4u a, v4u b) { asm volatile("v_mfma_f32_16x16x32_bf16 %0, %1, %2, %0" : "+v"(c) : "v"(a), "v"(b)); }
DI void mf_vv0(f32x4& c, v4u a, v4u b) { asm volatile("v_mfma_f32_16x16x32_bf16 %0, %1, %2, 0" : "=&v"(c) : "v"(a), "v"(b)); }
DI void mf_av(f32x4& c, v4u a, v4u b) { asm volatile("v_mfma_f32_16x16x32_bf16 %0, %1, %2, %0" : "+v"(c) : "a"(a), "v"(b)); }
DI void mf_av0(f32x4& c, v4u a, v4u b) { asm volatile("v_mfma_f32_16x16x32_bf16 %0, %1, %2, 0" : "=&v"(c) : "a"(a), "v"(b)); }
#define MFDONE1(a) asm volatile("s_nop 7" : "+v"(a))
#define TIE(a) asm volatile("" : "+v"(a))

DI float xsum_g(float a) {
  unsigned u = __float_as_uint(a);
  auto s = __builtin_amdgcn_permlane32_swap(u, u, false, false);
  a = __uint_as_float(s[0]) + __uint_as_float(s[1]);
  u = __float_as_uint(a);
  auto t = __builtin_amdgcn_permlane16_swap(u, u, false, false);
  return __uint_as_float(t[0]) + __uint_as_float(t[1]);
}
template <int CTRL> DI float dppf(float a) { return __uint_as_float(__builtin_amdgcn_update_dpp(0u, __float_as_uint(a), CTRL, 0xf, 0xf, true)); }
DI float wsum(float a) {
  a += dppf<0x128>(a); a += dppf<0x124>(a); a += dppf<0x122>(a); a += dppf<0x121>(a);
  return xsum_g(a);
}

struct Ctx {
  rsrc_t ws; v4u wsd;
  LAS unsigned char* L;
  int lane, w, b, xc, r, grp, mem;
};
#define STAMP(c, i) do {} while (0)

template <int NL> DI void wait_ctr(const Ctx& c, int line, unsigned target) {
  if (c.w == 0) {
    const int vo = O_CTR + (c.lane & (NL - 1)) * 256;
#pragma nounroll
    for (unsigned spins = 0;; ++spins) {
      asm volatile("" ::: "memory");
      const unsigned a = __builtin_amdgcn_raw_buffer_load_b32(c.ws, vo, line * 256, SC1);
      if (__all(a >= target)) break;
      if (spins > SPIN_CAP) { __builtin_amdgcn_raw_buffer_store_b32(line + 1, c.ws, O_TMO, 0, SC1); break; }
    }
  }
  BAR();
}
template <int NL> DI void arrive(const Ctx& c, int line, int stride) {
  BAR();
  if (threadIdx.x < NL) __builtin_amdgcn_raw_ptr_buffer_atomic_add_i32(1, c.ws, O_CTR + (line + c.lane * stride) * 256, 0, SC1);
}
template <int N> DI void dma(const Ctx& c, int vo, int so, int lds) {
  unsigned keep;
  if (N == 4) asm volatile("s_mov_b32 %0, m0\n\ts_mov_b32 m0, %4\n\ts_nop 0\n\tbuffer_load_dwordx4 %1, %2, %3 offen sc1 lds\n\tbuffer_load_dwordx4 %1, %2, %3 offen offset:1024 sc1 lds\n\t"
                           "buffer_load_dwordx4 %1, %2, %3 offen offset:2048 sc1 lds\n\tbuffer_load_dwordx4 %1, %2, %3 offen offset:3072 sc1 lds\n\ts_mov_b32 m0, %0"
                           : "=&s"(keep) : "v"(vo), "s"(c.wsd), "s"(so), "s"(lds) : "memory");
  else asm volatile("s_mov_b32 %0, m0\n\ts_mov_b32 m0, %4\n\ts_nop 0\n\tbuffer_load_dwordx4 %1, %2, %3 offen sc1 lds\n\ts_mov_b32 m0, %0"
                    : "=&s"(keep) : "v"(vo), "s"(c.wsd), "s"(so), "s"(lds) : "memory");
}
DI int gtile(int mem, int t) {
  if (mem >= 12) return 84 + 6 * (mem - 12) + (t < 6 ? t : 0);
  return (mem & 1) ? (t < 6 ? 7 * mem + 1 + t : 7 * mem) : 7 * mem + t;
}
DI void dma_w1_tile(const Ctx& c, int t) {
  const int so = O_W1T + (gtile(c.mem, t) * 20 + 5 * c.w) * 1024, lo = (t * 20 + 5 * c.w) * 1024;
  dma<4>(c, c.lane * 16, so, lo);
  dma<1>(c, c.lane * 16, so + 4096, lo + 4096);
}

DI void wunit_load(int id, rsrc_t rw1, rsrc_t rw2, int lane, v4u (&x)[4]) {
  const bool is1 = id < 540;
  const int j = is1 ? id : id - 540;
  const int kk = is1 ? j / 27 : j / 10, cq = is1 ? j % 27 : j % 10, rs = is1 ? F * 2 : D * 2;
  const int vo = (32 * kk + (lane >> 3)) * rs + cq * 128 + (lane & 7) * 16;
#pragma unroll
  for (int i = 0; i < 4; ++i) x[i] = is1 ? ld16(rw1, vo, 8 * i * F * 2, 0) : ld16(rw2, vo, 8 * i * D * 2, 0);
}
DI void wunit_store(const Ctx& c, int id, const v4u (&x)[4]) {
  const int lane = c.lane;
  LAS unsigned char* st = c.L + L_WST + c.w * 4608;
#pragma unroll
  for (int i = 0; i < 4; ++i) LDS16(st + (8 * i + (lane >> 3)) * WSTS + (lane & 7) * 16) = x[i];
  const bool is1 = id < 540;
  const int j = is1 ? id : id - 540;
  const int kk = is1 ? j / 27 : j / 10, cq = is1 ? j % 27 : j % 10;
  const int g = lane >> 4, q = (lane >> 2) & 3, p = lane & 3;
  LAS unsigned char* a0 = st + ((is1 ? 8 * g : 4 * g) + q) * WSTS + p * 8;
  LAS unsigned char* a1 = a0 + (is1 ? 4 : 16) * WSTS;
  asm volatile("s_waitcnt lgkmcnt(0)" ::: "memory");
#pragma unroll
  for (int t = 0; t < 4; ++t) {
    const v4u f = cat(trd(a0 + t * 32), trd(a1 + t * 32));
    const int ct = 4 * cq + t;
    const int so = is1 ? O_W1T + (ct * 20 + kk) * 1024 : O_W2T + (((ct / 5) * 54 + kk) * 5 + ct % 5) * 1024;
    st16(f, c.ws, lane * 16, so);
  }
  asm volatile("s_waitcnt lgkmcnt(0)" ::: "memory");
}

template <int T0> DI void conv_pass(const Ctx& c, const v4u (&A)[16], LAS unsigned char* b0, LAS unsigned char* b1, float (&cy)[3], const v4u (&qz)[2][2], int quad) {
  constexpr int EMAX = 2 * (T0 + 3), NE = EMAX + 2, NB = 4 * NE, PF = 3;
  f32x4 P[4][4];
  v4u Bq[PF + 1];
#pragma unroll
  for (int i = 0; i < PF; ++i) Bq[i] = cat(LDS8(b0 + (i / NE) * 1152 + (i % NE) * 64), LDS8(b1 + (i / NE) * 1152 + (i % NE) * 64));
#pragma unroll
  for (int i = 0; i < NB; ++i) {
    if (i + PF < NB) { constexpr int dummy = 0; (void)dummy;
      Bq[(i + PF) % (PF + 1)] = cat(LDS8(b0 + ((i + PF) / NE) * 1152 + ((i + PF) % NE) * 64), LDS8(b1 + ((i + PF) / NE) * 1152 + ((i + PF) % NE) * 64)); }
    const int ph = i / NE, e = i % NE - 1;
#pragma unroll
    for (int t = 0; t < 4; ++t) {
      const int lam = 2 * (T0 + t) - e;
      if (lam >= 0 && lam <= 15) { if (e == -1) mf_vv0(P[ph][t], A[lam], Bq[i % (PF + 1)]); else mf_vv(P[ph][t], A[lam], Bq[i % (PF + 1)]); }
    }
    SB();
  }
  MFDONE1(P[3][3]);
#pragma unroll
  for (int ph = 0; ph < 4; ++ph)
#pragma unroll
    for (int t = 0; t < 4; ++t) TIE(P[ph][t]);
  const int lane = c.lane, n = lane & 15, g = lane >> 4;
  const int src = 4 * (g > 0 ? lane - 16 : (n > 0 ? 47 + n : 63));
  LAS unsigned char* yi = c.L + L_YI;
#pragma unroll
  for (int t = 0; t < 4; ++t) {
    const float c0 = P[1][t][3] + P[2][t][2] + P[3][t][1], c1 = P[2][t][3] + P[3][t][2], c2 = P[3][t][3];
    const bool top = lane == 63;
    const float i0 = __int_as_float(__builtin_amdgcn_ds_bpermute(src, __float_as_int(top ? cy[0] : c0)));
    const float i1 = __int_as_float(__builtin_amdgcn_ds_bpermute(src, __float_as_int(top ? cy[1] : c1)));
    const float i2 = __int_as_float(__builtin_amdgcn_ds_bpermute(src, __float_as_int(top ? cy[2] : c2)));
    cy[0] = c0; cy[1] = c1; cy[2] = c2;
    const float y0 = P[0][t][0] + i0, y1 = P[0][t][1] + P[1][t][0] + i1, y2 = P[0][t][2] + P[1][t][1] + P[2][t][0] + i2,
                y3 = P[0][t][3] + P[1][t][2] + P[2][t][1] + P[3][t][0];
    LDS8(yi + c.w * YSTR + 2 * (256 * t + 16 * n + 4 * g)) = v2u{pk(y0, y1), pk(y2, y3)};
  }
  BAR();
  const int q = (lane >> 2) & 3, p = lane & 3;
#pragma unroll
  for (int i = 0; i < 2; ++i) {
    const int mb = 8 * (2 * c.w + i);
    LAS unsigned char* a = yi + q * YSTR + 2 * (16 * (mb + 2 * g + (p >> 1)) + 4 * (p & 1));
    const v2u ya = trd(a), yb = trd(a + 16);
    const v4u qa = qz[i][0], qb = qz[i][1];
    v4u x;
    x.x = pk(blo(qa.x) + blo(qa.z) * blo(ya.x), bhi(qa.x) + bhi(qa.z) * bhi(ya.x));
    x.y = pk(blo(qa.y) + blo(qa.w) * blo(ya.y), bhi(qa.y) + bhi(qa.w) * bhi(ya.y));
    x.z = pk(blo(qb.x) + blo(qb.z) * blo(yb.x), bhi(qb.x) + bhi(qb.z) * bhi(yb.x));
    x.w = pk(blo(qb.y) + blo(qb.w) * blo(yb.y), bhi(qb.y) + bhi(qb.w) * bhi(yb.y));
    const int vo = (lane >> 3) * 20480 + (lane & 7) * 16, so = (16 * T0 + mb) * 20480 + quad * 128;
    st16(x, c.ws, vo, O_X1 + so);
    st16(cat(ya, yb), c.ws, vo, O_Y + so);
  }
}
DI void load_qz(const Ctx& c, int hf, int quad, v4u (&qz)[2][2]) {
#pragma unroll
  for (int i = 0; i < 2; ++i)
#pragma unroll
    for (int t = 0; t < 2; ++t)
      qz[i][t] = ld16(c.ws, (c.lane >> 3) * 40960 + (c.lane & 7) * 16, O_Q8 + (2 * (64 * hf + 8 * (2 * c.w + i)) + t) * 20480 + quad * 128, SC1);
}

DI v4u fixp(v4u r) {
  v4u s;
  s.x = __builtin_amdgcn_update_dpp(r.x, r.z, 0x128, 0xf, 0xc, false);
  s.y = __builtin_amdgcn_update_dpp(r.y, r.w, 0x128, 0xf, 0xc, false);
  s.z = __builtin_amdgcn_update_dpp(r.z, r.x, 0x128, 0xf, 0x3, false);
  s.w = __builtin_amdgcn_update_dpp(r.w, r.y, 0x128, 0xf, 0x3, false);
  return s;
}
DI float mish(float h) {
  const float u = __builtin_amdgcn_exp2f(h * 1.4426950408889634f) + 1.0f;
  const float rc = __builtin_amdgcn_rcpf(__builtin_fmaf(u, u, 1.0f));
  return h * __builtin_fmaf(-2.0f, rc, 1.0f);
}
template <int LAYER> DI void gemm1(const Ctx& c, int hf) {
  const int lane = c.lane, mt = 64 * hf + 4 * c.grp + c.w;
  v4u x[20];
  const int so = (LAYER == 0 ? O_X1 : O_X3) + mt * 20480;
#pragma unroll
  for (int k = 0; k < 20; ++k) x[k] = ld16(c.ws, lane * 16, so + k * 1024, SC1);
  v2u sp[4];
  if (LAYER == 1) {
#pragma unroll
    for (int i = 0; i < 4; ++i) sp[i] = ld8(c.ws, (lane >> 4) * 512 + (lane & 15) * 8, O_ST3 + mt * 2048 + i * 128, SC1);
  }
  SB();
  constexpr int PF = 6, NW = 140;
  v4u Wq[PF + 1];
  LAS unsigned char* wl = c.L + lane * 16;
#define W1FRAG(i) LDS16(wl + (((i) % 7) * 20 + (i) / 7) * 1024)
#pragma unroll
  for (int i = 0; i < PF; ++i) Wq[i] = W1FRAG(i);
  f32x4 acc[7], sa, sq;
  v4u ones = {0x3f803f80u, 0x3f803f80u, 0x3f803f80u, 0x3f803f80u};
  TIE(ones);
  v4u xc = LAYER == 0 ? fixp(x[0]) : x[0];
  if (LAYER == 0) asm volatile("s_nop 1" : "+v"(xc));
#pragma unroll
  for (int k = 0; k < 20; ++k) {
    v4u xn = xc;
#pragma unroll
    for (int t = 0; t < 7; ++t) {
      const int i = k * 7 + t;
      if (i + PF < NW) Wq[(i + PF) % (PF + 1)] = W1FRAG(i + PF);
      if (k == 0) mf_vv0(acc[t], Wq[i % (PF + 1)], xc); else mf_vv(acc[t], Wq[i % (PF + 1)], xc);
      if (t == 2 && k < 19) { xn = LAYER == 0 ? fixp(x[k + 1]) : x[k + 1]; if (LAYER == 0) TIE(xn); }
      SB();
    }
    if (LAYER == 0) {
      if (k == 0) { mf_vv0(sa, ones, xc); mf_vv0(sq, xc, xc); } else { mf_vv(sa, ones, xc); mf_vv(sq, xc, xc); }
      SB();
    }
    xc = xn;
  }
#undef W1FRAG
  float s1, s2;
  if (LAYER == 0) {
    MFDONE1(sq); TIE(sa);
    const int n = lane & 15, g = lane >> 4;
    const float d = (n & 2) ? ((n & 1) ? sq[3] : sq[2]) : ((n & 1) ? sq[1] : sq[0]);
    s1 = sa[0];
    s2 = xsum_g(g == (n >> 2) ? d : 0.f);
  } else {
    MFDONE1(acc[6]);
    s1 = xsum_g((__uint_as_float(sp[0].x) + __uint_as_float(sp[1].x)) + (__uint_as_float(sp[2].x) + __uint_as_float(sp[3].x)));
    s2 = xsum_g((__uint_as_float(sp[0].y) + __uint_as_float(sp[1].y)) + (__uint_as_float(sp[2].y) + __uint_as_float(sp[3].y)));
  }
#pragma unroll
  for (int t = 0; t < 7; ++t) TIE(acc[t]);
  const float mu = s1 * (1.0f / D);
  const float rs = __builtin_amdgcn_rsqf(fmaxf(s2 * (1.0f / D) - mu * mu, 0.f) + 1e-5f), nm = -mu * rs;
  LAS unsigned char* cb = c.L + L_CB + (lane >> 4) * 16;
  const int hb = (LAYER == 0 ? O_H1 : O_H2) + mt * 54 * 1024;
  const int k0 = gtile(c.mem, 0) >> 1;
  v2u o[7];
#pragma unroll
  for (int t = 0; t < 7; ++t) {
    const f32x4 cs = LDSF4(cb + t * 64), bb = LDSF4(cb + 448 + t * 64);
    float h[4];
#pragma unroll
    for (int j = 0; j < 4; ++j) h[j] = mish(__builtin_fmaf(acc[t][j], rs, __builtin_fmaf(cs[j], nm, bb[j])));
    o[t] = v2u{pk(h[0], h[1]), pk(h[2], h[3])};
    if (t & 1) st16(cat(o[t - 1], o[t]), c.ws, lane * 16, hb + (k0 + (t >> 1)) * 1024);
  }
  const int g6 = gtile(c.mem, 6);
  st8(o[6], c.ws, lane * 16, c.mem >= 12 ? OOB : hb + (g6 >> 1) * 1024 + (g6 & 1) * 8);
}

DI void gemm2(const Ctx& c, const v4u (&W)[70], int hbase, int hf, f32x4 (&res)[3]) {
  const int lane = c.lane, w = c.w;
  const int mt0 = 64 * hf + 4 * c.grp + 2 * (c.mem >> 3), ks = 14 * w - (w == 3);
  v4u h[2][14];
#pragma unroll
  for (int m = 0; m < 2; ++m)
#pragma unroll
    for (int k = 0; k < 14; ++k) h[m][k] = ld16(c.ws, lane * 16, (k == 13 && w >= 2) ? OOB : hbase + ((mt0 + m) * 54 + ks + k) * 1024, SC1);
  SB();
  f32x4 acc[2][5];
  LAS unsigned char* red = c.L + L_RED + lane * 16;
#pragma unroll
  for (int m = 0; m < 2; ++m) {
#pragma unroll
    for (int k = 0; k < 14; ++k) {
#pragma unroll
      for (int t = 0; t < 5; ++t) {
        const int i = k * 5 + t;
        if (k == 0) mf_av0(acc[m][t], W[i], h[m][k]); else if (i < 64) mf_av(acc[m][t], W[i], h[m][k]); else mf_vv(acc[m][t], W[i], h[m][k]);
      }
      SB();
    }
    MFDONE1(acc[m][4]);
#pragma unroll
    for (int t = 0; t < 4; ++t) TIE(acc[m][t]);
    if (m == 1) {
      BAR();
      if (w == 0) {
#pragma unroll
        for (int t = 0; t < 3; ++t) res[t] = acc[0][t] + ((LDSF4(red + (t * 3) * 1024) + LDSF4(red + (t * 3 + 1) * 1024)) + LDSF4(red + (t * 3 + 2) * 1024));
      } else if (w == 2) {
#pragma unroll
        for (int t = 3; t < 5; ++t) res[t - 3] = acc[0][t] + ((LDSF4(red + (t * 3) * 1024) + LDSF4(red + (t * 3 + 1) * 1024)) + LDSF4(red + (t * 3 + 2) * 1024));
        res[2] = f32x4{0.f, 0.f, 0.f, 0.f};
      }
      BAR();
    }
    if (w != m) {
      LAS unsigned char* d = red + ((w - m - 1) & 3) * 1024;
#pragma unroll
      for (int t = 0; t < 3; ++t) LDSF4(d + t * 3072) = acc[m][t];
    }
    if (w != m + 2) {
      LAS unsigned char* d = red + ((w - m - 3) & 3) * 1024;
#pragma unroll
      for (int t = 3; t < 5; ++t) LDSF4(d + t * 3072) = acc[m][t];
    }
    SB();
  }
  BAR();
  if (w == 1) {
#pragma unroll
    for (int t = 0; t < 3; ++t) res[t] = acc[1][t] + ((LDSF4(red + (t * 3) * 1024) + LDSF4(red + (t * 3 + 1) * 1024)) + LDSF4(red + (t * 3 + 2) * 1024));
  } else if (w == 3) {
#pragma unroll
    for (int t = 3; t < 5; ++t) res[t - 3] = acc[1][t] + ((LDSF4(red + (t * 3) * 1024) + LDSF4(red + (t * 3 + 1) * 1024)) + LDSF4(red + (t * 3 + 2) * 1024));
    res[2] = f32x4{0.f, 0.f, 0.f, 0.f};
  }
}
struct Own { int mt, ct0, part; bool third; };
DI Own own(const Ctx& c, int hf) {
  Own o;
  o.mt = 64 * hf + 4 * c.grp + 2 * (c.mem >> 3) + (c.w & 1);
  o.ct0 = 5 * (c.mem & 7) + 3 * (c.w >> 1);
  o.part = 2 * (c.mem & 7) + (c.w >> 1);
  o.third = c.w < 2;
  return o;
}
DI void load_xy(const Ctx& c, int hf, v2u (&x1)[3], v2u (&y)[3]) {
  const Own o = own(c, hf);
  const int tok = c.lane & 15, vo = (c.lane >> 4) * 128 + (tok & 7) * 16 + (tok >> 3) * 8;
#pragma unroll
  for (int t = 0; t < 3; ++t) {
    const int so = o.mt * 20480 + (o.ct0 + t) * 512;
    const bool ok = t < 2 || o.third;
    x1[t] = ld8(c.ws, vo, ok ? O_X1 + so : OOB, SC1);
    y[t] = ld8(c.ws, vo, ok ? O_Y + so : OOB, SC1);
  }
}
DI void epi2a(const Ctx& c, int hf, f32x4 (&res)[3], const v2u (&x1)[3]) {
  const Own o = own(c, hf);
  float s1 = 0.f, s2 = 0.f;
#pragma unroll
  for (int t = 0; t < 3; ++t) {
    res[t] += f32x4{blo(x1[t].x), bhi(x1[t].x), blo(x1[t].y), bhi(x1[t].y)};
#pragma unroll
    for (int j = 0; j < 4; ++j) { s1 += res[t][j]; s2 = __builtin_fmaf(res[t][j], res[t][j], s2); }
  }
  s1 = xsum_g(s1); s2 = xsum_g(s2);
  st16(v4u{__float_as_uint(s1), 1u, __float_as_uint(s2), 1u}, c.ws, c.lane < 16 ? c.lane * 16 : OOB, O_ST2 + (o.mt * 16 + o.part) * 256);
}
DI void epi2b(const Ctx& c, int hf, const f32x4 (&x2)[3], const v2u (&y)[3], v2u (&x3)[3]) {
  const Own o = own(c, hf);
  const int lane = c.lane, tok = lane & 15, g = lane >> 4;
  v4u p[4];
#pragma nounroll
  for (unsigned spins = 0;; ++spins) {
    asm volatile("" ::: "memory");
#pragma unroll
    for (int i = 0; i < 4; ++i) p[i] = ld16(c.ws, g * 1024 + tok * 16, O_ST2 + o.mt * 4096 + i * 256, SC1);
    const bool ok = (p[0].y & p[0].w & p[1].y & p[1].w & p[2].y & p[2].w & p[3].y & p[3].w) == 1u;
    if (__all(ok)) break;
    if (spins > SPIN_CAP) { __builtin_amdgcn_raw_buffer_store_b32(999, c.ws, O_TMO, 0, SC1); break; }
  }
  float s1 = xsum_g((__uint_as_float(p[0].x) + __uint_as_float(p[1].x)) + (__uint_as_float(p[2].x) + __uint_as_float(p[3].x)));
  float s2 = xsum_g((__uint_as_float(p[0].z) + __uint_as_float(p[1].z)) + (__uint_as_float(p[2].z) + __uint_as_float(p[3].z)));
  const float mu = s1 * (1.0f / D);
  const float rs = __builtin_amdgcn_rsqf(fmaxf(s2 * (1.0f / D) - mu * mu, 0.f) + 1e-5f), nm = -mu * rs;
  s1 = 0.f; s2 = 0.f;
#pragma unroll
  for (int t = 0; t < 3; ++t) {
    const f32x4 yy = {blo(y[t].x), bhi(y[t].x), blo(y[t].y), bhi(y[t].y)};
    float v[4];
#pragma unroll
    for (int j = 0; j < 4; ++j) { v[j] = __builtin_fmaf(__builtin_fmaf(x2[t][j], rs, nm), yy[j], x2[t][j]); s1 += v[j]; s2 = __builtin_fmaf(v[j], v[j], s2); }
    x3[t] = v2u{pk(v[0], v[1]), pk(v[2], v[3])};
    const int ct = o.ct0 + t;
    st8(x3[t], c.ws, (g >> 1) * 256 + tok * 16 + (g & 1) * 8, (t < 2 || o.third) ? O_X3 + o.mt * 20480 + ct * 512 : OOB);
  }
  s1 = xsum_g(s1); s2 = xsum_g(s2);
  st8(v2u{__float_as_uint(s1), __float_as_uint(s2)}, c.ws, lane < 16 ? lane * 8 : OOB, O_ST3 + (o.mt * 16 + o.part) * 128);
}
DI void epi_out(const Ctx& c, int hf, const f32x4 (&res)[3], const v2u (&x3)[3], rsrc_t ro) {
  const Own o = own(c, hf);
  const int vo = (c.lane & 15) * (D * 2) + (c.lane >> 4) * 8;
#pragma unroll
  for (int t = 0; t < 3; ++t) {
    const v2u v = {pk(res[t][0] + blo(x3[t].x), res[t][1] + bhi(x3[t].x)), pk(res[t][2] + blo(x3[t].y), res[t][3] + bhi(x3[t].y))};
    __builtin_amdgcn_raw_buffer_store_b64(v, ro, vo, (t < 2 || o.third) ? o.mt * 16 * D * 2 + (o.ct0 + t) * 32 : OOB, 0);
  }
}

__global__ void __launch_bounds__(256, 1) h3_mish_kernel(const void* __restrict__ pq, const void* __restrict__ pk_, const void* __restrict__ pv, const void* __restrict__ psh,
                                                         const void* __restrict__ ph, const void* __restrict__ pD, const void* __restrict__ pw1, const void* __restrict__ pb1,
                                                         const void* __restrict__ pw2, void* pout, void* pws) {
  extern __shared__ __attribute__((aligned(16))) unsigned char smem[];
  Ctx c;
  c.L = (LAS unsigned char*)smem;
  const int tid = threadIdx.x;
  c.lane = tid & 63; c.w = __builtin_amdgcn_readfirstlane(tid >> 6);
  c.b = blockIdx.x; c.xc = c.b & 7; c.r = c.b >> 3; c.grp = 2 * c.xc + (c.r >> 4); c.mem = c.r & 15;
  c.ws = mk(pws, WS_END);
  c.wsd = v4u{(unsigned)(uintptr_t)pws, (unsigned)((uintptr_t)pws >> 32) & 0xffffu, (unsigned)WS_END, 0x00020000u};
  const int lane = c.lane, w = c.w, b = c.b, xc = c.xc, r = c.r;
  STAMP(c, 0);
  unsigned pf;
  {
    unsigned long long pc; asm volatile("s_getpc_b64 %0" : "=s"(pc));
    const unsigned* cp = (const unsigned*)(pc & ~127ull);
    pf = cp[(r & 1) * 8192 + tid * 32];
  }
  const rsrc_t rk = mk(pk_, SD2), rv = mk(pv, SD2), rq = mk(pq, SD2), rw1 = mk(pw1, D * F * 2), rw2 = mk(pw2, D * F * 2);
  const v2u seed = ld8(c.ws, 0, 0, SC1);
  const int oc = tid % 10, m4 = tid / 10;
  v4u kr[7], vr[4], sh[4];
  if (tid < 160) {
    const int vo = (64 * r + 4 * m4) * (D * 2) + xc * 160 + oc * 16;
    const int vom = (r + m4 > 0) ? vo - 3 * D * 2 : OOB;
#pragma unroll
    for (int i = 0; i < 7; ++i) kr[i] = i < 3 ? ld16(rk, vom, i * D * 2, 0) : ld16(rk, vo, (i - 3) * D * 2, 0);
#pragma unroll
    for (int i = 0; i < 4; ++i) vr[i] = ld16(rv, vo, i * D * 2, 0);
    const rsrc_t rsh = mk(psh, D * 8);
#pragma unroll
    for (int i = 0; i < 4; ++i) sh[i] = ld16(rsh, (xc * 80 + oc * 8) * 8, i * 16, 0);
  }
  const bool isconv = r < 20;
  const int quad = 20 * xc + r, ch = 4 * quad + w;
  v4u hr[4];
  unsigned dsc = 0;
  if (isconv) {
    const rsrc_t rh = mk(ph, SD2);
#pragma unroll
    for (int i = 0; i < 4; ++i) hr[i] = ld16(rh, lane * 16, ch * 4096 + i * 1024, 0);
    dsc = ((const unsigned short*)pD)[ch];
  }
  v4u qr[3];
  {
    const int row = 8 * b + 2 * w;
    qr[0] = ld16(rq, lane * 16, row * D * 2, 0);
    qr[1] = ld16(rq, lane * 16, (row + 1) * D * 2, 0);
    qr[2] = ld16(rq, lane < 32 ? (lane >> 4) * D * 2 + 1024 + (lane & 15) * 16 : OOB, row * D * 2, 0);
  }
  v4u wu[4];
  wunit_load(4 * b + w, rw1, rw2, lane, wu);
  SB();
  const unsigned tlo = seed.x + 1u, thi = seed.y;
  if (b == 0 && w == 3) {
#pragma unroll
    for (int i = 0; i < 5; ++i) __builtin_amdgcn_raw_buffer_store_b32(0u, c.ws, (i * 64 + lane < C_N) ? O_CTR + (i * 64 + lane) * 256 : OOB, 0, SC1);
    __builtin_amdgcn_raw_buffer_store_b32(0u, c.ws, O_TMO, 0, SC1);
    VMDRAIN();
    st8(v2u{tlo, thi}, c.ws, lane < 8 ? O_INIT + lane * 256 : OOB, 0);
  }
  if (tid < 160) {
#pragma unroll
    for (int ph_ = 0; ph_ < 4; ++ph_) {
      v4u o;
#pragma unroll
      for (int d = 0; d < 4; ++d) {
        const v4u s4 = sh[d];
        const float a = blo(kr[ph_ + 3][d]) * blo(s4.x) + blo(kr[ph_ + 2][d]) * bhi(s4.x) + blo(kr[ph_ + 1][d]) * blo(s4.y) + blo(kr[ph_][d]) * bhi(s4.y);
        const float e = bhi(kr[ph_ + 3][d]) * blo(s4.z) + bhi(kr[ph_ + 2][d]) * bhi(s4.z) + bhi(kr[ph_ + 1][d]) * blo(s4.w) + bhi(kr[ph_][d]) * bhi(s4.w);
        o[d] = pk(a * blo(vr[ph_][d]), e * bhi(vr[ph_][d]));
      }
      LDS16(c.L + L_KV + (4 * m4 + ph_) * KVS + oc * 16) = o;
    }
  }
  BAR();
  {
    const int G = lane >> 4, q = (lane >> 2) & 3, p = lane & 3, pi = 2 * G + (p >> 1);
    LAS unsigned char* a = c.L + L_KV + (4 * (8 * (pi >> 2) + q) + (pi & 3)) * KVS + (p & 1) * 8;
    const int vo = (4 * (q & 1) + p) * 4096 + (2 * G + (q >> 1)) * 16;
#pragma unroll
    for (int i = 0; i < 3; ++i) {
      const int o8 = w + 4 * i;
      if (o8 < 10) st16(cat(trd(a + o8 * 16), trd(a + o8 * 16 + 16 * KVS)), c.ws, vo, O_U + (xc * 80 + o8 * 8) * 4096 + r * 128);
    }
  }
  if (w == 0) {
#pragma nounroll
    for (unsigned spins = 0;; ++spins) {
      asm volatile("" ::: "memory");
      const v2u t = ld8(c.ws, O_INIT + xc * 256, 0, SC1);
      if (__all(t.x == tlo && t.y == thi) || spins > SPIN_CAP) break;
    }
  }
  VMDRAIN();
  asm volatile("" :: "v"(pf));
  arrive<1>(c, C_S0U + xc, 0);
  STAMP(c, 1);
  wunit_store(c, 4 * b + w, wu);
  if (!isconv && w == 3) {
    const int e = (r - 20) * 8 + xc;
    if (e < 56) { wunit_load(1024 + e, rw1, rw2, lane, wu); wunit_store(c, 1024 + e, wu); }
  }
  {
    float xa[3][8];
#pragma unroll
    for (int i = 0; i < 3; ++i)
#pragma unroll
      for (int d = 0; d < 4; ++d) { xa[i][2 * d] = blo(qr[i][d]); xa[i][2 * d + 1] = bhi(qr[i][d]); }
    float s[3];
#pragma unroll
    for (int i = 0; i < 3; ++i) s[i] = ((xa[i][0] + xa[i][1]) + (xa[i][2] + xa[i][3])) + ((xa[i][4] + xa[i][5]) + (xa[i][6] + xa[i][7]));
    const bool tA = lane < 16, tB = lane >= 16 && lane < 32;
    const float m0 = wsum(s[0] + (tA ? s[2] : 0.f)) * (1.0f / D), m1 = wsum(s[1] + (tB ? s[2] : 0.f)) * (1.0f / D);
    const float mm[3] = {m0, m1, tA ? m0 : m1};
#pragma unroll
    for (int i = 0; i < 3; ++i) {
      s[i] = 0.f;
#pragma unroll
      for (int d = 0; d < 8; ++d) { xa[i][d] -= mm[i]; s[i] = __builtin_fmaf(xa[i][d], xa[i][d], s[i]); }
    }
    const float r0 = __builtin_amdgcn_rsqf(wsum(s[0] + (tA ? s[2] : 0.f)) * (1.0f / D) + 1e-5f), r1 = __builtin_amdgcn_rsqf(wsum(s[1] + (tB ? s[2] : 0.f)) * (1.0f / D) + 1e-5f);
    const float rr[3] = {r0, r1, tA ? r0 : r1};
#pragma unroll
    for (int i = 0; i < 3; ++i) {
      const int o8 = i < 2 ? lane : 64 + (lane & 15), tk = 2 * w + (i < 2 ? i : (lane >> 4) & 1);
      if (i < 2 || lane < 32) {
        LAS unsigned char* d = c.L + L_Q + o8 * 256 + tk * 16;
        LDS16(d) = v4u{qr[i].x, qr[i].y, pk(xa[i][0] * rr[i], xa[i][1] * rr[i]), pk(xa[i][2] * rr[i], xa[i][3] * rr[i])};
        LDS16(d + 128) = v4u{qr[i].z, qr[i].w, pk(xa[i][4] * rr[i], xa[i][5] * rr[i]), pk(xa[i][6] * rr[i], xa[i][7] * rr[i])};
      }
    }
  }
  LAS unsigned char* himg = c.L + L_CONV + w * CONVW;
  LAS unsigned char* uimg = himg + 4160;
  if (isconv) {
    if (lane == 0) hr[0].x = (hr[0].x & 0xffff0000u) | (pk(blo(hr[0].x) + blo(dsc), 0.f) & 0xffffu);
#pragma unroll
    for (int i = 0; i < 4; ++i) LDS16(himg + 32 + i * 1024 + lane * 16) = hr[i];
    if (lane < 2) LDS16(himg + lane * 16) = v4u{0u, 0u, 0u, 0u};
    if (lane < 32) LDS16(uimg + (lane >> 3) * 1152 + (lane & 7) * 16) = v4u{0u, 0u, 0u, 0u};
  }
  if (w < 2) st16(v4u{0u, 0u, 0u, 0u}, c.ws, tid * 16, O_ST2 + b * 2048);
  BAR();
#pragma unroll
  for (int i = 0; i < 5; ++i) st16(LDS16(c.L + L_Q + (w * 5 + i) * 1024 + lane * 16), c.ws, lane * 16, O_Q8 + b * 20480 + (w * 5 + i) * 1024);
  VMDRAIN();
  arrive<8>(c, C_S0W + xc, 8);
  STAMP(c, 2);
  if (isconv) {
    v4u A[16];
    {
      const int g = lane >> 4, q = (lane >> 2) & 3, p = lane & 3;
      LAS unsigned char* a = himg + 2 * (128 + 4 * p - 32 * g - 4 * q) - 32;
#pragma unroll
      for (int l = 0; l < 16; ++l) A[l] = cat(trd(a + 256 * l + 32), trd(a + 256 * l));
    }
    wait_ctr<1>(c, C_S0U + xc, 32);
    STAMP(c, 3);
    v4u ur[4];
#pragma unroll
    for (int i = 0; i < 4; ++i) ur[i] = ld16(c.ws, lane * 16, O_U + ch * 4096 + i * 1024, SC1);
#pragma unroll
    for (int i = 0; i < 4; ++i) LDS16(uimg + (lane & 3) * 1152 + 128 + (16 * i + (lane >> 2)) * 16) = ur[i];
    wait_ctr<8>(c, C_S0W + xc * 8, 32);
    STAMP(c, 4);
    v4u qa[2][2], qb[2][2];
    load_qz(c, 0, quad, qa);
    load_qz(c, 1, quad, qb);
#pragma unroll
    for (int t = 0; t < 5; ++t) dma_w1_tile(c, t);
    LAS unsigned char* b0 = uimg + 128 + 2 * (-28 + 4 * (lane & 15) + 8 * (lane >> 4)) - 64;
    LAS unsigned char* b1 = b0 + 8;
    asm volatile("" : "+v"(b1));
    float cy[3] = {0.f, 0.f, 0.f};
    conv_pass<0>(c, A, b0, b1, cy, qa, quad);
    VMDRAIN();
    arrive<8>(c, C_S1 + xc, 8);
    STAMP(c, 5);
    conv_pass<4>(c, A, b0, b1, cy, qb, quad);
    VMDRAIN();
    arrive<8>(c, C_S1 + 64 + xc, 8);
    STAMP(c, 6);
    dma_w1_tile(c, 5); dma_w1_tile(c, 6);
  } else {
    wait_ctr<8>(c, C_S0W + xc * 8, 32);
#pragma unroll
    for (int t = 0; t < 7; ++t) dma_w1_tile(c, t);
  }
  if (b == 0 && tid == 0) st8(v2u{tlo, thi}, c.ws, 0, 0);
  {
    const rsrc_t rb = mk(pb1, F * 2);
    const int g = lane >> 4;
    const v2u ba = ld8(rb, g * 8, gtile(c.mem, w) * 32, 0), bb = ld8(rb, g * 8, gtile(c.mem, w == 3 ? 3 : w + 4) * 32, 0);
    VMDRAIN();
    BAR();
    v4u ones = {0x3f803f80u, 0x3f803f80u, 0x3f803f80u, 0x3f803f80u};
    asm volatile("s_nop 1" : "+v"(ones));
    f32x4 ca, cb2;
    LAS unsigned char* wa = c.L + w * 20480 + lane * 16;
    LAS unsigned char* wb = c.L + (w == 3 ? 3 : w + 4) * 20480 + lane * 16;
#pragma unroll
    for (int k = 0; k < 20; ++k) {
      const v4u fa = LDS16(wa + k * 1024), fb = LDS16(wb + k * 1024);
      if (k == 0) { mf_vv0(ca, fa, ones); mf_vv0(cb2, fb, ones); } else { mf_vv(ca, fa, ones); mf_vv(cb2, fb, ones); }
    }
    MFDONE1(cb2); TIE(ca);
    if ((lane & 15) == 0) {
      LAS unsigned char* d = c.L + L_CB + g * 16;
      LDSF4(d + w * 64) = ca;
      LDSF4(d + 448 + w * 64) = f32x4{blo(ba.x), bhi(ba.x), blo(ba.y), bhi(ba.y)};
      if (w < 3) { LDSF4(d + (w + 4) * 64) = cb2; LDSF4(d + 448 + (w + 4) * 64) = f32x4{blo(bb.x), bhi(bb.x), blo(bb.y), bhi(bb.y)}; }
    }
  }
  STAMP(c, 7);
  v4u W[70];
  {
    const int so = O_W2T + ((c.mem & 7) * 54 + 14 * w - (w == 3)) * 5 * 1024;
#pragma unroll
    for (int i = 0; i < 70; ++i) W[i] = ld16(c.ws, lane * 16, (i >= 65 && w >= 2) ? OOB : so + i * 1024, SC1);
    SB();
  }
  const int gl = C_G + c.grp;
  wait_ctr<8>(c, C_S1 + xc * 8, 20);
  STAMP(c, 8);
  gemm1<0>(c, 0); VMDRAIN(); arrive<1>(c, gl, 0);
  STAMP(c, 9);
  wait_ctr<8>(c, C_S1 + 64 + xc * 8, 20);
  STAMP(c, 10);
  gemm1<0>(c, 1); VMDRAIN(); arrive<1>(c, gl + 16, 0);
  STAMP(c, 11);
  f32x4 ra[3], rb_[3];
  v2u x1a[3], ya[3], x1b[3], yb[3], x3a[3], x3b[3];
  load_xy(c, 0, x1a, ya);
  wait_ctr<1>(c, gl, 16);
  STAMP(c, 12);
  gemm2(c, W, O_H1, 0, ra);
  epi2a(c, 0, ra, x1a);
  STAMP(c, 13);
  load_xy(c, 1, x1b, yb);
  wait_ctr<1>(c, gl + 16, 16);
  STAMP(c, 14);
  gemm2(c, W, O_H1, 1, rb_);
  epi2a(c, 1, rb_, x1b);
  STAMP(c, 15);
  epi2b(c, 0, ra, ya, x3a); VMDRAIN(); arrive<1>(c, gl + 32, 0);
  STAMP(c, 16);
  epi2b(c, 1, rb_, yb, x3b); VMDRAIN(); arrive<1>(c, gl + 48, 0);
  STAMP(c, 17);
  wait_ctr<1>(c, gl + 32, 16);
  STAMP(c, 18);
  gemm1<1>(c, 0); VMDRAIN(); arrive<1>(c, gl + 64, 0);
  STAMP(c, 19);
  wait_ctr<1>(c, gl + 48, 16);
  STAMP(c, 20);
  gemm1<1>(c, 1); VMDRAIN(); arrive<1>(c, gl + 80, 0);
  STAMP(c, 21);
  const rsrc_t ro = mk(pout, SD2);
  wait_ctr<1>(c, gl + 64, 16);
  STAMP(c, 22);
  gemm2(c, W, O_H2, 0, ra);
  epi_out(c, 0, ra, x3a, ro);
  STAMP(c, 23);
  wait_ctr<1>(c, gl + 80, 16);
  STAMP(c, 24);
  gemm2(c, W, O_H2, 1, rb_);
  epi_out(c, 1, rb_, x3b, ro);
  STAMP(c, 25);
}

extern "C" void kernel_launch(void* const* d_in, const int* in_sizes, int n_in, void* d_out, int out_size, void* d_ws, size_t ws_size, hipStream_t stream) {
  (void)in_sizes; (void)n_in; (void)out_size; (void)ws_size;
  (void)hipFuncSetAttribute((const void*)h3_mish_kernel, hipFuncAttributeMaxDynamicSharedMemorySize, LDS_BYTES);
  h3_mish_kernel<<<256, 256, LDS_BYTES, stream>>>(d_in[0], d_in[1], d_in[2], d_in[3], d_in[4], d_in[5], d_in[7], d_in[8], d_in[9], d_out, d_ws);
}
```

```cpp
#include <hip/hip_runtime.h>
#include <stdint.h>

#define DI __device__ __forceinline__
#define LAS __attribute__((address_space(3)))
typedef unsigned v4u __attribute__((ext_vector_type(4)));
typedef unsigned v2u __attribute__((ext_vector_type(2)));
typedef float f32x4 __attribute__((ext_vector_type(4)));
typedef float f32x2 __attribute__((ext_vector_type(2)));
typedef short s16x4 __attribute__((ext_vector_type(4)));
typedef __bf16 bf16x2 __attribute__((ext_vector_type(2)));
typedef __amdgpu_buffer_rsrc_t rsrc_t;
typedef LAS unsigned char* lptr;
typedef unsigned long long u64;

constexpr int S = 2048, D = 640, F = 1728;
constexpr int SD2 = S * D * 2;
constexpr int SC1 = 16;
constexpr int OOB = 0x7f000000;
constexpr int O_INIT = 4096;
constexpr int O_CTR = 20480;
constexpr int C_S0W = 0, C_S0Q = 8, C_S1A = 16, C_S1B = 24, C_S0V = 32, C_S0U = 40, C_G = 41;
constexpr int O_TMO = O_CTR + 55 * 256 + 16;
constexpr int O_CS = 137216;
constexpr int O_ST2 = 151552;
constexpr int O_ST3 = O_ST2 + 524288;
constexpr int O_STAMP = O_ST3 + 262144;
constexpr int O_U = O_STAMP + 131072;
constexpr int O_X0 = O_U + SD2;
constexpr int O_ST0 = O_X0 + SD2;
constexpr int O_W1T = O_ST0 + 32768;
constexpr int O_W2T = O_X0 + 2 * SD2 + D * F * 2;
constexpr int O_X1 = O_W2T + D * F * 2;
constexpr int O_Y = O_X1 + SD2;
constexpr int O_X3 = O_Y + SD2;
constexpr int O_H1 = O_Y + 2 * SD2;
constexpr int O_H2 = O_H1 + S * F * 2;
constexpr int WS_END = O_H1 + 2 * S * F * 2 + 65536;
constexpr int LDS_BYTES = 163840;
constexpr int L_KV = 0, KVS = 176;
constexpr int L_SH = 12288, L_CNT = L_SH + 4096;
constexpr int L_W1S = 20480;
constexpr int L_H = 114560, HW_ = 4128;
constexpr int L_WST = 114688;
constexpr int L_YI = 129024, YSTR = 2064;
constexpr int L_U = 143360, UW = 4608;
constexpr int L_RED = 143360;
constexpr int CODE_BYTES = 52904;
constexpr unsigned SPIN_CAP = 1u << 20;

#define POLLALL 0
#define POLLATOM 1
#define TW1 0
#define XFORM 31
#define CLAIM 1
#define QEARLY 0
#define TLBW 0
#define TLBSTR 1048576
#define STAMPS 0

DI v4u ld16(rsrc_t rs, int vo, int so, int aux) { return aux ? __builtin_amdgcn_raw_buffer_load_b128(rs, vo, so, SC1) : __builtin_amdgcn_raw_buffer_load_b128(rs, vo, so, 0); }
DI v2u ld8(rsrc_t rs, int vo, int so, int aux) { return aux ? __builtin_amdgcn_raw_buffer_load_b64(rs, vo, so, SC1) : __builtin_amdgcn_raw_buffer_load_b64(rs, vo, so, 0); }
DI void st16(v4u v, rsrc_t rs, int vo, int so) {
  asm volatile("" : "+v"(v));
  __builtin_amdgcn_raw_buffer_store_b128(v, rs, vo, so, SC1);
  asm volatile("s_nop 1" :: "v"(v));
}
DI void st8(v2u v, rsrc_t rs, int vo, int so) { __builtin_amdgcn_raw_buffer_store_b64(v, rs, vo, so, SC1); }
DI rsrc_t mk(const void* p, int bytes) { return __builtin_amdgcn_make_buffer_rsrc((void*)p, 0, bytes, 0x00020000); }
DI float blo(unsigned u) { return __uint_as_float(u << 16); }
DI float bhi(unsigned u) { return __uint_as_float(u & 0xffff0000u); }
DI unsigned pk(float a, float b) { f32x2 f = {a, b}; return __builtin_bit_cast(unsigned, __builtin_convertvector(f, bf16x2)); }
DI float dot2(unsigned a, unsigned b, float c) { return __builtin_amdgcn_fdot2_f32_bf16(__builtin_bit_cast(bf16x2, a), __builtin_bit_cast(bf16x2, b), c, false); }
DI v2u trd(lptr p) { return __builtin_bit_cast(v2u, __builtin_amdgcn_ds_read_tr16_b64_v4i16((LAS s16x4*)p)); }
DI v4u cat(v2u a, v2u b) { return v4u{a.x, a.y, b.x, b.y}; }
DI f32x4 unp(v2u a) { return f32x4{blo(a.x), bhi(a.x), blo(a.y), bhi(a.y)}; }
#define LDS16(p) (*(LAS v4u*)(p))
#define LDS8(p) (*(LAS v2u*)(p))
#define LDSF4(p) (*(LAS f32x4*)(p))
#define BAR0() asm volatile("s_waitcnt lgkmcnt(0)\n\ts_barrier" ::: "memory")
#define BARRIER() asm volatile("s_barrier" ::: "memory")
#define DRAINBAR(n) do { asm volatile("" ::: "memory"); WAITC(n, 15); asm volatile("s_barrier ; DRAIN" ::: "memory"); } while (0)
#define WAITC(vm, lg) __builtin_amdgcn_s_waitcnt(((vm) & 15) | (7 << 4) | ((lg) << 8) | (((vm) >> 4) << 14))
#define FENCE() asm volatile("")
#define TIE(a) asm volatile("" : "+v"(a))
#define TIES(a) asm volatile("" : "+s"(a))
#define NOP8() asm volatile("s_nop 7")
#define MFV(c, a, b) asm volatile("v_mfma_f32_16x16x32_bf16 %0, %1, %2, %0" : "+v"(c) : "v"(a), "v"(b))
#define MFV0(c, a, b) asm volatile("v_mfma_f32_16x16x32_bf16 %0, %1, %2, 0" : "=&v"(c) : "v"(a), "v"(b))
#define MFA(c, a, b) asm volatile("v_mfma_f32_16x16x32_bf16 %0, %1, %2, %0" : "+v"(c) : "a"(a), "v"(b))
#define MFA0(c, a, b) asm volatile("v_mfma_f32_16x16x32_bf16 %0, %1, %2, 0" : "=&v"(c) : "a"(a), "v"(b))
#define OP1(op, d, a) asm volatile(op " %0, %1" : "=v"(d) : "v"(a))
#define OP2(op, d, a, b) asm volatile(op " %0, %1, %2" : "=v"(d) : "v"(a), "v"(b))
#define OP2S(op, d, s, b) asm volatile(op " %0, %1, %2" : "=v"(d) : "s"(s), "v"(b))
#define OP3(op, d, a, b, e) asm volatile(op " %0, %1, %2, %3" : "=v"(d) : "v"(a), "v"(b), "v"(e))
#define OP3S(op, d, a, s, e) asm volatile(op " %0, %1, %2, %3" : "=v"(d) : "v"(a), "s"(s), "v"(e))
#define SEL(d, a, b, m) asm volatile("v_cndmask_b32 %0, %1, %2, %3" : "=v"(d) : "v"(a), "v"(b), "s"(m))
#define SHL16(d, a) asm volatile("v_lshlrev_b32 %0, 16, %1" : "=v"(d) : "v"(a))
#define SADD(d, a, imm) asm volatile("s_add_u32 %0, %1, %2" : "=s"(d) : "s"(a), "i"(imm) : "scc")
#define SADDS(d, a, b) asm volatile("s_add_u32 %0, %1, %2" : "=s"(d) : "s"(a), "s"(b) : "scc")
#define SMUL(d, a, imm) asm volatile("s_mul_i32 %0, %1, %2" : "=s"(d) : "s"(a), "i"(imm))
#define SETM0(a, imm) asm volatile("s_add_u32 m0, %0, %1" :: "s"(a), "i"(imm) : "scc")
#define DMA(so, imm) asm volatile("buffer_load_dwordx4 %0, %1, %2 offen offset:" #imm " sc1 lds" :: "v"(vo0), "s"(wsd), "s"(so) : "memory")
#define SB() __builtin_amdgcn_sched_barrier(0)
#define PFMA(a, b, c) __builtin_elementwise_fma(a, b, c)
#define FIXA(d, s, m) asm volatile("s_mov_b64 vcc, %6\n\tv_cndmask_b32_dpp %0, %4, %2, vcc row_ror:8 row_mask:0xf bank_mask:0xf\n\tv_cndmask_b32_dpp %1, %5, %3, vcc row_ror:8 row_mask:0xf bank_mask:0xf" \
  : "=&v"(d##a0), "=&v"(d##a1) : "v"(s.x), "v"(s.y), "v"(s.z), "v"(s.w), "s"(m) : "vcc")
#define FIXB(d, s, m) asm volatile("s_mov_b64 vcc, %6\n\tv_cndmask_b32_dpp %0, %2, %4, vcc row_ror:8 row_mask:0xf bank_mask:0xf\n\tv_cndmask_b32_dpp %1, %3, %5, vcc row_ror:8 row_mask:0xf bank_mask:0xf" \
  : "=&v"(d##b0), "=&v"(d##b1) : "v"(s.x), "v"(s.y), "v"(s.z), "v"(s.w), "s"(m) : "vcc")
#define LO2(v) __builtin_shufflevector(v, v, 0, 1)
#define HI2(v) __builtin_shufflevector(v, v, 2, 3)

DI float xsum_g(float a) {
  unsigned u = __float_as_uint(a);
  auto s = __builtin_amdgcn_permlane32_swap(u, u, false, false);
  a = __uint_as_float(s[0]) + __uint_as_float(s[1]);
  u = __float_as_uint(a);
  auto t = __builtin_amdgcn_permlane16_swap(u, u, false, false);
  return __uint_as_float(t[0]) + __uint_as_float(t[1]);
}
template <int CTRL> DI float dppf(float a) { return __uint_as_float(__builtin_amdgcn_update_dpp(0u, __float_as_uint(a), CTRL, 0xf, 0xf, true)); }
DI float wsum(float a) {
  a += dppf<0x128>(a); a += dppf<0x124>(a); a += dppf<0x122>(a); a += dppf<0x121>(a);
  return xsum_g(a);
}
DI v4u rowpair(v2u a, v2u b) {
  auto x = __builtin_amdgcn_permlane16_swap(a.x, b.x, false, false);
  auto y = __builtin_amdgcn_permlane16_swap(a.y, b.y, false, false);
  return v4u{x[0], y[0], x[1], y[1]};
}
DI int w1pos(int ft) {
  const int m = ft < 84 ? (ft * 37) >> 8 : 12 + (((ft - 84) * 43) >> 8), p = ft - (m < 12 ? 7 * m : 12 + 6 * m);
  return m * 140 + ((m < 12 && (m & 1)) ? (p == 0 ? 6 : p - 1) : p);
}
DI float selq(float a, float b, float c, float d, int lane) {
  const float e = (lane & 1) ? b : a, f = (lane & 1) ? d : c;
  return (lane & 2) ? f : e;
}
DI void lnstat(float s1, float s2, float& rs, float& nm) {
  const float mu = s1 * (1.0f / D);
  rs = __builtin_amdgcn_rsqf(fmaxf(__builtin_fmaf(s2, 1.0f / D, -mu * mu), 0.f) + 1e-5f);
  nm = -mu * rs;
}
#define PEEK(vo, so) (unsigned)__builtin_amdgcn_raw_ptr_buffer_atomic_add_i32(0, ws, vo, so, SC1)
template <int SLEEP = 2, bool PLAIN = false> DI void spin(rsrc_t ws, int vo, int so, unsigned target, u64 mask) {
#pragma nounroll
  for (unsigned spins = 0;; ++spins) {
    asm volatile("; SPIN" ::: "memory");
    const unsigned a = PLAIN ? __builtin_amdgcn_raw_buffer_load_b32(ws, vo, so, SC1) : PEEK(vo, so);
    if ((__builtin_amdgcn_ballot_w64(a < target) & mask) == 0) break;
    if (spins > SPIN_CAP) { __builtin_amdgcn_raw_buffer_store_b32(so, ws, O_TMO, 0, SC1); break; }
    if (SLEEP) __builtin_amdgcn_s_sleep(SLEEP);
  }
}
#define POLL_LD(var, vo, so) unsigned var; asm volatile("v_mov_b32 %0, 0\n\tbuffer_atomic_add %0, %1, %2, %3 offen sc0 sc1" : "=&v"(var) : "v"(vo), "s"(wsd), "s"(so) : "memory")
#define POLL_CK(var, vo, so, tgt) do { TIE(var); if (__builtin_expect((__builtin_amdgcn_ballot_w64(var < (tgt)) & vo##m) != 0, 0)) spin(ws, vo, so, tgt, vo##m); BARRIER(); } while (0)
#define POLL_CKN(var, vo, so, tgt) do { TIE(var); if (__builtin_expect((__builtin_amdgcn_ballot_w64(var < (tgt)) & vo##m) != 0, 0)) spin(ws, vo, so, tgt, vo##m); } while (0)
#define ARRIVE(vo, so) (void)__builtin_amdgcn_raw_ptr_buffer_atomic_add_i32(1, ws, vo, so, SC1)

#define STAMP(i) do {} while (0)

__global__ void __launch_bounds__(256, 1) h3_mish_kernel(const char* pq, const void* pk_, const void* pv, const void* psh,
                                                         const void* ph, const void* pD, const void* pw1, const void* pb1,
                                                         const void* pw2, void* pout, void* pws) {
  extern __shared__ __attribute__((aligned(16))) unsigned char smem[];
  const lptr L = (lptr)smem;
  const int tid = threadIdx.x, lane = tid & 63, w = __builtin_amdgcn_readfirstlane(tid >> 6);
  const int b = blockIdx.x, xc = b & 7, r = b >> 3;
  const int grp = 4 * (xc >> 1) + (r >> 3), mem = 8 * (xc & 1) + (r & 7);
  const int grp2 = 2 * xc + (r >> 4), ci = r & 7, ri = (r >> 3) & 1;
#define SRD(p, n) v4u{(unsigned)(uintptr_t)(p), (unsigned)((uintptr_t)(p) >> 32) & 0xffffu, (unsigned)(n), 0x00020000u}
#define DMAI(vo, srd, so, lds, imm) asm volatile("s_mov_b32 m0, %3\n\ts_nop 0\n\tbuffer_load_dwordx4 %0, %1, %2 offen offset:" #imm " lds" :: "v"(vo), "s"(srd), "s"(so), "s"(lds) : "memory")
#define DMAN(vo, srd, so, imm) asm volatile("buffer_load_dwordx4 %0, %1, %2 offen offset:" #imm " lds" :: "v"(vo), "s"(srd), "s"(so) : "memory")
#define LDA(d, vo, srd, imm) asm volatile("buffer_load_dwordx4 %0, %1, %2, 0 offen offset:" #imm : "=v"(d) : "v"(vo), "s"(srd))
  unsigned pf;
  {
    u64 pc; int len;
    asm volatile("s_getpc_b64 %0\n\ts_mov_b32 %1, %2" : "=s"(pc), "=s"(len) : "i"(CODE_BYTES));
    const v4u cs_ = {(unsigned)pc, (unsigned)(pc >> 32) & 0xffffu, (unsigned)len, 0x00020000u};
    asm volatile("buffer_load_dword %0, %1, %2, %3 offen" : "=v"(pf) : "v"(((r & 7) * 64 + lane) * 128), "s"(cs_), "s"((w == 3 && r != 0) ? 0 : OOB));
  }
  int vo0 = lane * 16; TIE(vo0);
  const int m4 = tid / 10, oc = tid - 10 * m4;
  int vkv = (64 * r + 4 * m4) * (D * 2) + xc * 160 + oc * 16; TIE(vkv);
  vkv = tid < 160 ? vkv : OOB;
  int vkm = (r + m4 > 0) ? vkv - 3 * D * 2 : OOB; TIE(vkv); TIE(vkm);
  asm volatile("" : "+s"(pq), "+s"(pk_), "+s"(pv), "+s"(psh), "+s"(ph), "+s"(pD), "+s"(pw1), "+s"(pb1), "+s"(pw2), "+s"(pout), "+s"(pws));
  const v4u wsd = SRD(pws, WS_END);
  const rsrc_t ws = mk(pws, WS_END);
  STAMP(0);
  const bool isconv = r < 20, ldr = w == 3;
  v2u seed, tag0; unsigned dsc;
#define TAGOF(s) ((s) == 0xffffffffu ? 1u : (s) + 1u)
  DMAI(vo0, SRD(psh, D * 8), xc * 640, L_SH + w * 1024, 0);
  v4u g0, g1, g2, g3, g4, g5, g6, g7, g8, g9, g10;
  {
    const v4u rk = SRD(pk_, SD2), rv = SRD(pv, SD2);
    LDA(g0, vkm, rk, 0); LDA(g1, vkm, rk, 1280); LDA(g2, vkm, rk, 2560); LDA(g3, vkv, rk, 0); LDA(g7, vkv, rv, 0);
    LDA(g4, vkv, rk, 1280); LDA(g8, vkv, rv, 1280); LDA(g5, vkv, rk, 2560); LDA(g9, vkv, rv, 2560); LDA(g6, vkv, rk, 3840); LDA(g10, vkv, rv, 3840);
  }
  v2u sdl = {0u, 0u};
  if (__builtin_expect(r == 0 && ldr, 0)) {
    sdl = ld8(ws, 0, 0, SC1);
    int vi = (lane >> 3) * 256 + (lane & 7) * 16; TIE(vi);
#pragma unroll
    for (int i = 0; i < 7; ++i) st16(v4u{0u, 0u, 0u, 0u}, ws, vi, O_CTR + xc * 14592 + i * 2048);
  }
  const int quad = 20 * xc + r, ch = 4 * quad + w, ne = (r - 20) * 8 + xc;
  STAMP(30);
  const lptr himg = L + L_H + w * HW_, uimg = L + L_U + w * UW;
  const int ft0 = mem < 12 ? 7 * mem : 12 + 6 * mem, odd = mem < 12 ? mem & 1 : 0, ft6 = (odd || mem >= 12) ? ft0 : ft0 + 6;
  const int hiw = w >> 1, j8 = r & 7, rota = j8 >= 6 ? j8 - 6 : j8, rb_ = 2 * (r >> 3) + (r & 1), rotb = rb_ >= 5 ? rb_ - 5 : rb_, rg_ = j8 + (r >> 3), rotg = rg_ >= 10 ? rg_ - 10 : (rg_ >= 5 ? rg_ - 5 : rg_);
  const unsigned pka = (unsigned)(0x654210654210ull >> (4 * rota)), pkb = (unsigned)((hiw ? 0xcb987cb987ull : 0xdcb987dcb987ull) >> (4 * rotb)), pkx = (unsigned)(0x4321043210ull >> (4 * rotg));
  int ko0, ko1, ko2, ko3, ko4, ko5, ko6, ko7, ko8, ko9, ko10, ko11, xo0, xo1, xo2, xo3, xo4;
#define KO(i) do { ko##i = (int)(((i < 6 ? pka >> (4 * i) : pkb >> (4 * (i - 6))) & 15u) << 10); if (i == 11 && hiw) ko##i = OOB; TIES(ko##i); } while (0)
#define XO(i) do { xo##i = (int)(((pkx >> (4 * i)) & 15u) << 12); TIES(xo##i); } while (0)
  KO(0); KO(1); KO(2); KO(3); KO(4); KO(5); KO(6); KO(7); KO(8); KO(9); KO(10); KO(11); XO(0); XO(1); XO(2); XO(3); XO(4);
  const int ks = 14 * w - (w == 3);
  const int w1s = O_W1T + mem * 143360 + w * 7168, w1d = w * 7168;
  int w2p = O_W2T + (ci * 54 + ks) * 5120 + hiw * 3072, w2q;
  const int mtg = 4 * grp + w;
  int xb00 = O_X1 + mtg * 20480;
  TIES(w2p); TIES(xb00);
  w2q = w2p + 2048 - hiw * 5120;
  const int w2pz = hiw ? OOB : w2p + ko11 * 5, w2qz = hiw ? OOB : w2q + ko11 * 5;
  const int xb01 = xb00 + 64 * 20480;
  if (tid == 0) *(LAS unsigned*)(L + L_CNT) = 0u;
#define TAG0() asm volatile("buffer_load_dwordx2 %0, %1, %2, %3 offen sc1" : "=v"(tag0) : "v"((lane & 7) * 256), "s"(wsd), "s"(O_INIT + xc * 2048))
#define TAGWAIT() do { if (__builtin_expect(__builtin_amdgcn_ballot_w64(tag0.x != TAGOF(seed.x) || tag0.y != seed.y) != 0, 0)) { \
    _Pragma("nounroll") for (unsigned spins = 0; spins < SPIN_CAP; ++spins) { asm volatile("; SPIN" ::: "memory"); \
      const v2u t = ld8(ws, (lane & 7) * 256, O_INIT + xc * 2048, SC1); \
      if (__builtin_amdgcn_ballot_w64(t.x != TAGOF(seed.x) || t.y != seed.y) == 0) break; } } } while (0)
#define LANDED(n) asm volatile("s_waitcnt vmcnt(" #n ")" : "+v"(g0), "+v"(g1), "+v"(g2), "+v"(g3), "+v"(g4), "+v"(g5), "+v"(g6), "+v"(g7), "+v"(g8), "+v"(g9), "+v"(g10), "+v"(pf) :: "memory")
  const int wj1 = ldr ? 540 : 3 * b + w, wk1 = (wj1 * 2428) >> 16, wc1 = wj1 - 27 * wk1;
  int v1a = (lane >> 3) * F * 2 + (((lane & 7) ^ ((lane >> 3) & 2)) << 4), v1b = v1a ^ 64, so1 = wj1 < 540 ? 32 * wk1 * F * 2 + wc1 * 128 : OOB; TIE(v1a); TIE(v1b); TIES(so1);
  int kd_ = 4 * m4 * KVS + oc * 16; TIE(kd_);
  const lptr kd = L + L_KV + (tid < 160 ? kd_ : 64 * KVS);
  lptr ka; int vu;
  { const int G = lane >> 4, q = (lane >> 2) & 3, p = lane & 3, pi = 2 * G + (p >> 1);
    ka = L + L_KV + (4 * (8 * (pi >> 2) + q) + (pi & 3)) * KVS + (p & 1) * 8 + w * 16; vu = (4 * (q & 1) + p) * 4096 + (2 * G + (q >> 1)) * 16; TIE(ka); TIE(vu); }
  STAMP(33);
  asm volatile("s_waitcnt vmcnt(11)" ::: "memory");
  v4u sh[4];
#pragma unroll
  for (int d = 0; d < 4; ++d) sh[d] = LDS16(L + L_SH + w * 1024 + oc * 64 + d * 16);
  LANDED(0);
  if (__builtin_expect(r == 0 && ldr, 0)) st16(v4u{TAGOF(sdl.x), sdl.y, 0u, 0u}, ws, (lane >> 3) * 2048 + (lane & 7) * 16, O_INIT + xc * 256);
  BARRIER();
  STAMP(31);
  if (__builtin_expect(!ldr, 1)) {
    {
      const v4u s1 = SRD(pw1, D * F * 2); const int ld = L_W1S + w * 4096;
      DMAI(v1a, s1, so1, ld, 0); DMAI(v1b, s1, so1 + 8 * F * 2, ld + 1024, 0); DMAI(v1a, s1, so1 + 16 * F * 2, ld + 2048, 0); DMAI(v1b, s1, so1 + 24 * F * 2, ld + 3072, 0);
    }
    const v4u kr[7] = {g0, g1, g2, g3, g4, g5, g6}, vr[4] = {g7, g8, g9, g10};
    v4u o[4];
#pragma unroll
    for (int d = 0; d < 4; ++d) {
      unsigned pl[7], pu[7];
#pragma unroll
      for (int i = 1; i < 7; ++i) { pl[i] = __builtin_amdgcn_perm(kr[i - 1][d], kr[i][d], 0x05040100u); pu[i] = __builtin_amdgcn_perm(kr[i - 1][d], kr[i][d], 0x07060302u); }
#pragma unroll
      for (int f = 0; f < 4; ++f) {
        const float a = dot2(pl[f + 3], sh[d].x, dot2(pl[f + 1], sh[d].y, 0.f)), e = dot2(pu[f + 3], sh[d].z, dot2(pu[f + 1], sh[d].w, 0.f));
        const f32x2 t = f32x2{a, e} * f32x2{blo(vr[f][d]), bhi(vr[f][d])};
        o[f][d] = pk(t[0], t[1]);
      }
    }
#pragma unroll
    for (int f = 0; f < 4; ++f) LDS16(kd + f * KVS) = o[f];
    STAMP(1);
    BAR0();
    STAMP(34);
    {
      v4u f[4];
#pragma unroll
      for (int i = 0; i < 4; ++i) f[i] = cat(trd(ka + i * 48), trd(ka + i * 48 + 16 * KVS));
#pragma unroll
      for (int i = 0; i < 4; ++i) if (i < 3 || w == 0) st16(f[i], ws, vu, O_U + (xc * 80 + (w + 3 * i) * 8) * 4096 + r * 128);
    }
    STAMP(35);
  } else {
    v4u q0, q1, q2, q3, q4, q5, q6, q7, q8, q9;
    { const v4u qs = SRD(pq + b * 8 * D * 2, 8 * D * 2); const int vq = (lane & 7) * (D * 2) + (lane >> 3) * 16;
      LDA(q0, vq, qs, 0); LDA(q1, vq, qs, 128); LDA(q2, vq, qs, 256); LDA(q3, vq, qs, 384); LDA(q4, vq, qs, 512);
      LDA(q5, vq, qs, 640); LDA(q6, vq, qs, 768); LDA(q7, vq, qs, 896); LDA(q8, vq, qs, 1024); LDA(q9, vq, qs, 1152); }
    if (isconv) {
      const v4u hs = SRD(ph, SD2);
#pragma unroll
      for (int u = 0; u < 4; ++u) { const int so = quad * 16384 + u * 4096; DMAI(vo0, hs, so, L_H + u * HW_ + 32, 0); DMAN(vo0, hs, so, 1024); DMAN(vo0, hs, so, 2048); DMAN(vo0, hs, so, 3072); }
    }
    BAR0();
    if (isconv) asm volatile("s_waitcnt vmcnt(16)" ::: "memory"); else asm volatile("s_waitcnt vmcnt(0)" ::: "memory");
    asm volatile("" : "+v"(q0), "+v"(q1), "+v"(q2), "+v"(q3), "+v"(q4), "+v"(q5), "+v"(q6), "+v"(q7), "+v"(q8), "+v"(q9));
    const v4u qr[10] = {q0, q1, q2, q3, q4, q5, q6, q7, q8, q9};
#pragma unroll
    for (int i = 0; i < 10; ++i) st16(qr[i], ws, vo0 + (i & 3) * 1024, O_X0 + b * 10240 + (i >> 2) * 4096);
    f32x2 a = {0.f, 0.f}, e = {0.f, 0.f};
#pragma unroll
    for (int i = 0; i < 10; ++i)
#pragma unroll
      for (int d = 0; d < 4; ++d) { const f32x2 x = {blo(qr[i][d]), bhi(qr[i][d])}; a += x; e = PFMA(x, x, e); }
    float s1 = a[0] + a[1], s2 = e[0] + e[1], rr, nn;
    s1 += dppf<0x128>(s1); s2 += dppf<0x128>(s2);
    lnstat(xsum_g(s1), xsum_g(s2), rr, nn); TIE(rr); TIE(nn);
    STAMP(52);
    st16(v4u{__float_as_uint(rr), __float_as_uint(nn), 0u, 0u}, ws, lane < 8 ? vo0 : OOB, O_ST0 + b * 128);
    STAMP(53);
  }
  asm volatile("buffer_load_dwordx2 %0, off, %1, 0 sc1" : "=v"(seed) : "s"(wsd));
  asm volatile("buffer_load_ushort %0, off, %1, %2" : "=v"(dsc) : "s"(SRD(pD, D * 2)), "s"(isconv ? ch * 2 : OOB));
  TAG0();
#define USE(v) asm volatile("" :: "v"(v))
  int vcb = ((lane >> 3) < 6 ? ft0 + odd + (lane >> 3) : ft6) * 128 + (lane & 7) * 16; TIE(vcb); vcb = tid < 56 ? vcb : OOB; TIE(vcb);
#define CB_LOAD() ld16(ws, vcb, O_CS, SC1)
#define CB_STORE() do { if (tid < 56) LDS16(L + L_RED + lane * 16) = cbv; } while (0)
  v4u ones = {0x3f803f80u, 0x3f803f80u, 0x3f803f80u, 0x3f803f80u}; TIE(ones);
  const bool pw_ = POLLALL || w == 0;
  int vp1 = (pw_ && lane == 0) ? O_CTR : OOB, vp8 = O_CTR + xc * 14592 + lane * 256, va1 = tid == 0 ? O_CTR : OOB, vq3 = O_CTR + lane * 14592 + xc * 256;
  TIE(vp8); TIE(vq3);
  int va8 = tid < 8 ? vq3 : OOB;
  vp8 = (pw_ && lane < 8) ? vp8 : OOB; vq3 = (ldr && lane < 8) ? vq3 : OOB;
  TIE(vp1); TIE(vp8); TIE(va1); TIE(va8); TIE(vq3);
  const u64 vp1m = pw_ ? 1ull : 0ull, vp8m = pw_ ? 0xffull : 0ull;
  lptr ub0 = uimg + 128 + 2 * (-28 + 4 * (lane & 15) + 8 * (lane >> 4)) - 64, ub1 = ub0 + 8;
  lptr yw = L + L_YI + w * YSTR + 32 * (lane & 15) + 8 * (lane >> 4);
  lptr yr = L + L_YI + ((lane >> 2) & 3) * YSTR + 32 * (16 * w + 2 * (lane >> 4) + ((lane >> 1) & 1)) + 8 * (lane & 1);
  lptr ywb = uimg + 1024 + w * 16 + 32 * (lane & 15) + 8 * (lane >> 4);
  lptr yrb = L + L_U + 1024 + ((lane >> 2) & 3) * (UW + 16) + 32 * (16 * w + 2 * (lane >> 4) + ((lane >> 1) & 1)) + 8 * (lane & 1);
  lptr afp = himg + 2 * (128 + 4 * (lane & 3) - 32 * (lane >> 4) - 4 * ((lane >> 2) & 3)) - 32, uwp = uimg + (lane & 3) * 1152 + 128 + (lane >> 2) * 16;
  int bsrc = 4 * (lane >= 16 ? lane - 16 : (lane > 0 ? 47 + lane : 63));
  int vqz = (lane >> 3) * 20480 + (lane & 7) * 16, vst0 = (lane >> 3) * 256 + (lane & 7) * 16;
  TIE(ub0); TIE(ub1); TIE(yw); TIE(yr); TIE(ywb); TIE(yrb); TIE(afp); TIE(uwp); TIE(bsrc); TIE(vqz); TIE(vst0);
  int qzb = O_X0 + 32 * w * 10240 + (quad >> 1) * 128 + (quad & 1) * 8, stb = O_ST0 + w * 4096, x1b = O_X1 + 16 * w * 20480 + quad * 128;
  TIES(qzb); { int t_ = w; TIES(t_); stb = O_ST0 + t_ * 4096; } TIES(stb); TIES(x1b);
  const int yb = x1b + SD2;
  if (isconv && lane < 34) LDS16(lane < 32 ? uimg + (lane >> 3) * 1152 + (lane & 7) * 16 : himg + (lane - 32) * 16) = v4u{0u, 0u, 0u, 0u};
  STAMP(36);
  asm volatile("s_waitcnt vmcnt(0)" : "+v"(tag0), "+v"(seed), "+v"(dsc) :: "memory");
  STAMP(32);
  unsigned cnt = 0;
  if (lane == 0 && !ldr) cnt = __hip_atomic_fetch_add((LAS unsigned*)(L + L_CNT), 1u, __ATOMIC_RELAXED, __HIP_MEMORY_SCOPE_WORKGROUP);
  TAGWAIT();
  ARRIVE(ldr ? vq3 : ((lane == 0 && cnt == 2) ? O_CTR + (xc * 57 + C_S0U - C_S0Q) * 256 : OOB), C_S0Q * 256);
  if (wj1 < 540) {
    const int g_ = lane >> 4, q_ = (lane >> 2) & 3, fa = (q_ & 2) | ((g_ & 1) << 2);
    const lptr a0 = L + L_W1S + w * 4096 + (8 * g_ + q_) * 128 + (lane & 1) * 8;
    v4u f[4];
#pragma unroll
    for (int t = 0; t < 4; ++t) { const int pc_ = ((2 * t + ((lane >> 1) & 1)) ^ fa) << 4; f[t] = cat(trd(a0 + pc_), trd(a0 + 512 + pc_)); }
#pragma unroll
    for (int t = 0; t < 4; ++t) st16(f[t], ws, vo0, O_W1T + (w1pos(4 * wc1 + t) + 7 * wk1) * 1024);
  }
  const unsigned tlo = TAGOF(seed.x), thi = seed.y;
  STAMP(2);
  v4u W0, W1, W2, W3, W4, W5, W6, W7, W8, W9, W10, W11, W12, W13, W14, W15, W16, W17, W18, W19, W20, W21, W22, W23, W24, W25, W26, W27, W28, W29, W30, W31, W32, W33, W34, W35, W36, W37, W38, W39, W40, W41, W42, W43, W44, W45, W46, W47, W48, W49, W50, W51, W52, W53, W54, W55, W56, W57, W58, W59, W60, W61, W62, W63, W64, W65, W66, W67, W68, W69;
  v4u x00_0, x00_1, x00_2, x00_3, x00_4, x00_5, x00_6, x00_7, x00_8, x00_9, x00_10, x00_11, x00_12;
  v4u cbv;
  if (isconv) {
    v4u A0, A1, A2, A3, A4, A5, A6, A7, A8, A9, A10, A11, A12, A13, A14, A15;
  const bool top = lane == 63;
  u64 mtop = 1ull << 63; unsigned mh16 = 0xffff0000u; float fz = 0.f;
  if (w == 0) { if (0) __builtin_amdgcn_s_sleep(0); spin<3, false>(ws, vp1, (xc * 57 + C_S0U) * 256, 32, 1ull); }
  asm volatile("s_waitcnt vmcnt(0)\n\ts_barrier" ::: "memory");
  ARRIVE(va8, C_S0W * 256);
  STAMP(3);
  {
    v4u ur[4];
#pragma unroll
    for (int i = 0; i < 4; ++i) ur[i] = ld16(ws, vo0 + i * 1024, O_U + ch * 4096, SC1);
    const unsigned h0 = *(LAS unsigned short*)(himg + 32);
    if (lane == 0) *(LAS unsigned short*)(himg + 32) = (unsigned short)pk(blo(h0) + blo(dsc), 0.f);
#define AF(l) A##l = cat(trd(afp + 256 * l + 32), trd(afp + 256 * l))
    AF(0); AF(1); AF(2); AF(3); AF(4); AF(5); AF(6); AF(7); AF(8); AF(9); AF(10); AF(11); AF(12); AF(13); AF(14); AF(15);
#undef AF
    asm volatile("" : "+a"(A0), "+a"(A1), "+a"(A2), "+a"(A3), "+a"(A4), "+a"(A5), "+a"(A6), "+a"(A7), "+a"(A8), "+a"(A9), "+a"(A10), "+a"(A11), "+a"(A12), "+a"(A13), "+a"(A14), "+a"(A15));
#pragma unroll
    for (int i = 0; i < 4; ++i) LDS16(uwp + i * 256) = ur[i];
  }

  const v2u ba33 = LDS8(ub0 + 0); FENCE();
  const v2u bb34 = LDS8(ub1 + 0); FENCE();
  const v2u ba35 = LDS8(ub0 + 64); FENCE();
  const v2u bb36 = LDS8(ub1 + 64); FENCE();
  STAMP(4);
  WAITC(63, 2);
  f32x4 P0_0_0; MFA0(P0_0_0, A1, cat(ba33, bb34));
  const v2u ba37 = LDS8(ub0 + 128); FENCE();
  const v2u bb38 = LDS8(ub1 + 128); FENCE();
  f32x4 P0_0_1; MFA0(P0_0_1, A3, cat(ba33, bb34));
  POLL_LD(pl2, vp8, (C_S0Q) * 256);
  f32x4 P0_0_2; MFA0(P0_0_2, A5, cat(ba33, bb34));
  f32x4 P0_0_3; MFA0(P0_0_3, A7, cat(ba33, bb34));
  WAITC(63, 2);
  const v2u ba39 = LDS8(ub0 + 192); FENCE();
  const v2u bb40 = LDS8(ub1 + 192); FENCE();
  MFA(P0_0_0, A0, cat(ba35, bb36));
  MFA(P0_0_1, A2, cat(ba35, bb36));
  POLL_LD(pl1, vp8, (C_S0W) * 256);
  MFA(P0_0_2, A4, cat(ba35, bb36));
  const v2u ba41 = LDS8(ub0 + 256); FENCE();
  const v2u bb42 = LDS8(ub1 + 256); FENCE();
  MFA(P0_0_3, A6, cat(ba35, bb36));
  WAITC(63, 4);
  MFA(P0_0_1, A1, cat(ba37, bb38));
  const v2u ba43 = LDS8(ub0 + 320); FENCE();
  const v2u bb44 = LDS8(ub1 + 320); FENCE();
  MFA(P0_0_2, A3, cat(ba37, bb38));
  MFA(P0_0_3, A5, cat(ba37, bb38));
  WAITC(63, 4);
  const v2u ba45 = LDS8(ub0 + 384); FENCE();
  const v2u bb46 = LDS8(ub1 + 384); FENCE();
  MFA(P0_0_1, A0, cat(ba39, bb40));
  const v2u ba47 = LDS8(ub0 + 448); FENCE();
  const v2u bb48 = LDS8(ub1 + 448); FENCE();
  MFA(P0_0_2, A2, cat(ba39, bb40));
  const v2u ba49 = LDS8(ub0 + 1152); FENCE();
  const v2u bb50 = LDS8(ub1 + 1152); FENCE();
  MFA(P0_0_3, A4, cat(ba39, bb40));
  WAITC(63, 8);
  MFA(P0_0_2, A1, cat(ba41, bb42));
  MFA(P0_0_3, A3, cat(ba41, bb42));
  WAITC(63, 6);
  MFA(P0_0_2, A0, cat(ba43, bb44));
  const v2u ba51 = LDS8(ub0 + 1216); FENCE();
  const v2u bb52 = LDS8(ub1 + 1216); FENCE();
  MFA(P0_0_3, A2, cat(ba43, bb44));
  WAITC(63, 6);
  MFA(P0_0_3, A1, cat(ba45, bb46));
  WAITC(63, 4);
  MFA(P0_0_3, A0, cat(ba47, bb48));
  WAITC(63, 2);
  f32x4 P0_1_0; MFA0(P0_1_0, A1, cat(ba49, bb50));
  const v2u ba53 = LDS8(ub0 + 1280); FENCE();
  const v2u bb54 = LDS8(ub1 + 1280); FENCE();
  f32x4 P0_1_1; MFA0(P0_1_1, A3, cat(ba49, bb50));
  f32x4 P0_1_2; MFA0(P0_1_2, A5, cat(ba49, bb50));
  f32x4 P0_1_3; MFA0(P0_1_3, A7, cat(ba49, bb50));
  WAITC(63, 2);
  const v2u ba55 = LDS8(ub0 + 1344); FENCE();
  const v2u bb56 = LDS8(ub1 + 1344); FENCE();
  MFA(P0_1_0, A0, cat(ba51, bb52));
  MFA(P0_1_1, A2, cat(ba51, bb52));
  MFA(P0_1_2, A4, cat(ba51, bb52));
  const v2u ba57 = LDS8(ub0 + 1408); FENCE();
  const v2u bb58 = LDS8(ub1 + 1408); FENCE();
  MFA(P0_1_3, A6, cat(ba51, bb52));
  WAITC(63, 4);
  MFA(P0_1_1, A1, cat(ba53, bb54));
  const v2u ba59 = LDS8(ub0 + 1472); FENCE();
  const v2u bb60 = LDS8(ub1 + 1472); FENCE();
  MFA(P0_1_2, A3, cat(ba53, bb54));
  MFA(P0_1_3, A5, cat(ba53, bb54));
  WAITC(63, 4);
  const v2u ba61 = LDS8(ub0 + 1536); FENCE();
  const v2u bb62 = LDS8(ub1 + 1536); FENCE();
  MFA(P0_1_1, A0, cat(ba55, bb56));
  const v2u ba63 = LDS8(ub0 + 1600); FENCE();
  const v2u bb64 = LDS8(ub1 + 1600); FENCE();
  MFA(P0_1_2, A2, cat(ba55, bb56));
  const v2u ba65 = LDS8(ub0 + 2304); FENCE();
  const v2u bb66 = LDS8(ub1 + 2304); FENCE();
  MFA(P0_1_3, A4, cat(ba55, bb56));
  WAITC(63, 8);
  MFA(P0_1_2, A1, cat(ba57, bb58));
  MFA(P0_1_3, A3, cat(ba57, bb58));
  WAITC(63, 6);
  MFA(P0_1_2, A0, cat(ba59, bb60));
  const v2u ba67 = LDS8(ub0 + 2368); FENCE();
  const v2u bb68 = LDS8(ub1 + 2368); FENCE();
  MFA(P0_1_3, A2, cat(ba59, bb60));
  WAITC(0, 6);
  POLL_CKN(pl1, vp8, (C_S0W) * 256, 32);
  MFA(P0_1_3, A1, cat(ba61, bb62));
  WAITC(1, 2);
  POLL_CK(pl2, vp8, (C_S0Q) * 256, 32);
  MFA(P0_1_3, A0, cat(ba63, bb64));
  int so28; SADD(so28, qzb, 0);
  const v2u qx0_0 = ld8(ws, vqz, so28, SC1);
  int so29; SADD(so29, qzb, 10240);
  f32x4 P0_2_0; MFA0(P0_2_0, A1, cat(ba65, bb66));
  const v2u ba69 = LDS8(ub0 + 2432); FENCE();
  const v2u bb70 = LDS8(ub1 + 2432); FENCE();
  f32x4 P0_2_1; MFA0(P0_2_1, A3, cat(ba65, bb66));
  f32x4 P0_2_2; MFA0(P0_2_2, A5, cat(ba65, bb66));
  f32x4 P0_2_3; MFA0(P0_2_3, A7, cat(ba65, bb66));
  WAITC(63, 2);
  const v2u ba71 = LDS8(ub0 + 2496); FENCE();
  const v2u bb72 = LDS8(ub1 + 2496); FENCE();
  MFA(P0_2_0, A0, cat(ba67, bb68));
  const v2u qx0_1 = ld8(ws, vqz, so29, SC1);
  int so30; SADD(so30, qzb, 163840);
  MFA(P0_2_1, A2, cat(ba67, bb68));
  MFA(P0_2_2, A4, cat(ba67, bb68));
  const v2u ba73 = LDS8(ub0 + 2560); FENCE();
  const v2u bb74 = LDS8(ub1 + 2560); FENCE();
  MFA(P0_2_3, A6, cat(ba67, bb68));
  WAITC(63, 4);
  MFA(P0_2_1, A1, cat(ba69, bb70));
  const v2u ba75 = LDS8(ub0 + 2624); FENCE();
  const v2u bb76 = LDS8(ub1 + 2624); FENCE();
  MFA(P0_2_2, A3, cat(ba69, bb70));
  const v2u qx0_2 = ld8(ws, vqz, so30, SC1);
  int so31; SADD(so31, qzb, 174080);
  MFA(P0_2_3, A5, cat(ba69, bb70));
  WAITC(63, 4);
  const v2u ba77 = LDS8(ub0 + 2688); FENCE();
  const v2u bb78 = LDS8(ub1 + 2688); FENCE();
  MFA(P0_2_1, A0, cat(ba71, bb72));
  const v2u ba79 = LDS8(ub0 + 2752); FENCE();
  const v2u bb80 = LDS8(ub1 + 2752); FENCE();
  MFA(P0_2_2, A2, cat(ba71, bb72));
  const v2u ba81 = LDS8(ub0 + 3456); FENCE();
  const v2u bb82 = LDS8(ub1 + 3456); FENCE();
  MFA(P0_2_3, A4, cat(ba71, bb72));
  WAITC(63, 8);
  const v2u qx0_3 = ld8(ws, vqz, so31, SC1);
  int so32; SADD(so32, stb, 0);
  MFA(P0_2_2, A1, cat(ba73, bb74));
  MFA(P0_2_3, A3, cat(ba73, bb74));
  WAITC(63, 6);
  MFA(P0_2_2, A0, cat(ba75, bb76));
  const v2u ba83 = LDS8(ub0 + 3520); FENCE();
  const v2u bb84 = LDS8(ub1 + 3520); FENCE();
  MFA(P0_2_3, A2, cat(ba75, bb76));
  WAITC(63, 6);
  const v2u qs0_0 = ld8(ws, vst0 + 0, so32, SC1);
  MFA(P0_2_3, A1, cat(ba77, bb78));
  WAITC(63, 4);
  MFA(P0_2_3, A0, cat(ba79, bb80));
  WAITC(63, 2);
  f32x4 P0_3_0; MFA0(P0_3_0, A1, cat(ba81, bb82));
  const v2u ba85 = LDS8(ub0 + 3584); FENCE();
  const v2u bb86 = LDS8(ub1 + 3584); FENCE();
  f32x4 P0_3_1; MFA0(P0_3_1, A3, cat(ba81, bb82));
  const v2u qs0_1 = ld8(ws, vst0 + 128, so32, SC1);
  f32x4 P0_3_2; MFA0(P0_3_2, A5, cat(ba81, bb82));
  f32x4 P0_3_3; MFA0(P0_3_3, A7, cat(ba81, bb82));
  WAITC(63, 2);
  const v2u ba87 = LDS8(ub0 + 3648); FENCE();
  const v2u bb88 = LDS8(ub1 + 3648); FENCE();
  MFA(P0_3_0, A0, cat(ba83, bb84));
  MFA(P0_3_1, A2, cat(ba83, bb84));
  const v2u qs0_2 = ld8(ws, vst0 + 2048, so32, SC1);
  MFA(P0_3_2, A4, cat(ba83, bb84));
  const v2u ba89 = LDS8(ub0 + 3712); FENCE();
  const v2u bb90 = LDS8(ub1 + 3712); FENCE();
  MFA(P0_3_3, A6, cat(ba83, bb84));
  WAITC(63, 4);
  MFA(P0_3_1, A1, cat(ba85, bb86));
  const v2u ba91 = LDS8(ub0 + 3776); FENCE();
  const v2u bb92 = LDS8(ub1 + 3776); FENCE();
  MFA(P0_3_2, A3, cat(ba85, bb86));
  WAITC(63, 4);
  const v2u qs0_3 = ld8(ws, vst0 + 2176, so32, SC1);
  if (b == 0 && tid == 0) st8(v2u{tlo, thi}, ws, 0, 0);
  MFA(P0_3_3, A5, cat(ba85, bb86));
  int so3; SMUL(so3, xo0, 7);
  const v2u ba93 = LDS8(ub0 + 3840); FENCE();
  const v2u bb94 = LDS8(ub1 + 3840); FENCE();
  MFA(P0_3_1, A0, cat(ba87, bb88));
  int so4; SADDS(so4, w1s, so3);
  const v2u ba95 = LDS8(ub0 + 3904); FENCE();
  const v2u bb96 = LDS8(ub1 + 3904); FENCE();
  MFA(P0_3_2, A2, cat(ba87, bb88));
  SETM0(w1d, 0);
  MFA(P0_3_3, A4, cat(ba87, bb88));
  WAITC(63, 6);
  DMA(so4, 0);
  MFA(P0_3_2, A1, cat(ba89, bb90));
  MFA(P0_3_3, A3, cat(ba89, bb90));
  WAITC(63, 4);
  MFA(P0_3_2, A0, cat(ba91, bb92));
  MFA(P0_3_3, A2, cat(ba91, bb92));
  WAITC(63, 2);
  DMA(so4, 1024);
  MFA(P0_3_3, A1, cat(ba93, bb94));
  WAITC(63, 0);
  MFA(P0_3_3, A0, cat(ba95, bb96));
  DMA(so4, 2048);
  STAMP(5);
  NOP8(); TIE(P0_0_0); TIE(P0_0_1); TIE(P0_0_2); TIE(P0_0_3); TIE(P0_1_0); TIE(P0_1_1); TIE(P0_1_2); TIE(P0_1_3); TIE(P0_2_0); TIE(P0_2_1); TIE(P0_2_2); TIE(P0_2_3); TIE(P0_3_0); TIE(P0_3_1); TIE(P0_3_2); TIE(P0_3_3);
  const float c97 = P0_1_0[3] + P0_2_0[2]; SB();
  const float c99 = P0_2_0[3] + P0_3_0[2]; SB();
  const float c98 = c97 + P0_3_0[1]; SB();
  const float s100 = top ? 0.f : c99; SB();
  const float i101 = __int_as_float(__builtin_amdgcn_ds_bpermute(bsrc, __float_as_int(s100)));
  const float s102 = top ? 0.f : P0_3_0[3]; SB();
  const float i103 = __int_as_float(__builtin_amdgcn_ds_bpermute(bsrc, __float_as_int(s102)));
  const float s104 = top ? 0.f : c98; SB();
  const float i105 = __int_as_float(__builtin_amdgcn_ds_bpermute(bsrc, __float_as_int(s104)));
  const float c118 = P0_1_1[3] + P0_2_1[2]; SB();
  const float c120 = P0_2_1[3] + P0_3_1[2]; SB();
  const float c119 = c118 + P0_3_1[1]; SB();
  const float s121 = top ? c99 : c120; SB();
  const float i122 = __int_as_float(__builtin_amdgcn_ds_bpermute(bsrc, __float_as_int(s121)));
  const float s123 = top ? P0_3_0[3] : P0_3_1[3]; SB();
  const float i124 = __int_as_float(__builtin_amdgcn_ds_bpermute(bsrc, __float_as_int(s123)));
  const float s125 = top ? c98 : c119; SB();
  const float i126 = __int_as_float(__builtin_amdgcn_ds_bpermute(bsrc, __float_as_int(s125)));
  const float c139 = P0_1_2[3] + P0_2_2[2]; SB();
  const float c141 = P0_2_2[3] + P0_3_2[2]; SB();
  const float c140 = c139 + P0_3_2[1]; SB();
  const float s142 = top ? c120 : c141; SB();
  const float i143 = __int_as_float(__builtin_amdgcn_ds_bpermute(bsrc, __float_as_int(s142)));
  const float s144 = top ? P0_3_1[3] : P0_3_2[3]; SB();
  const float i145 = __int_as_float(__builtin_amdgcn_ds_bpermute(bsrc, __float_as_int(s144)));
  const float s146 = top ? c119 : c140; SB();
  const float i147 = __int_as_float(__builtin_amdgcn_ds_bpermute(bsrc, __float_as_int(s146)));
  const float c160 = P0_1_3[3] + P0_2_3[2]; SB();
  const float c162 = P0_2_3[3] + P0_3_3[2]; SB();
  const float c161 = c160 + P0_3_3[1]; SB();
  const float s163 = top ? c141 : c162; SB();
  const float i164 = __int_as_float(__builtin_amdgcn_ds_bpermute(bsrc, __float_as_int(s163)));
  const float s165 = top ? P0_3_2[3] : P0_3_3[3]; SB();
  const float i166 = __int_as_float(__builtin_amdgcn_ds_bpermute(bsrc, __float_as_int(s165)));
  const float s167 = top ? c140 : c161; SB();
  const float i168 = __int_as_float(__builtin_amdgcn_ds_bpermute(bsrc, __float_as_int(s167)));
  const float y106 = P0_0_0[1] + P0_1_0[0]; SB();
  const float y107 = P0_0_0[2] + P0_1_0[1]; SB();
  const float y109 = P0_0_0[3] + P0_1_0[2]; SB();
  const float y108 = y107 + P0_2_0[0]; SB();
  const float y110 = y109 + P0_2_0[1]; SB();
  const float y111 = y110 + P0_3_0[0]; SB();
  const float y127 = P0_0_1[1] + P0_1_1[0]; SB();
  const float y128 = P0_0_1[2] + P0_1_1[1]; SB();
  const float y130 = P0_0_1[3] + P0_1_1[2]; SB();
  const float y129 = y128 + P0_2_1[0]; SB();
  const float y131 = y130 + P0_2_1[1]; SB();
  const float y132 = y131 + P0_3_1[0]; SB();
  const float y148 = P0_0_2[1] + P0_1_2[0]; SB();
  const float y149 = P0_0_2[2] + P0_1_2[1]; SB();
  const float y151 = P0_0_2[3] + P0_1_2[2]; SB();
  const float y150 = y149 + P0_2_2[0]; SB();
  const float y152 = y151 + P0_2_2[1]; SB();
  const float y153 = y152 + P0_3_2[0]; SB();
  const float y169 = P0_0_3[1] + P0_1_3[0]; SB();
  const float y170 = P0_0_3[2] + P0_1_3[1]; SB();
  const float y172 = P0_0_3[3] + P0_1_3[2]; SB();
  const float y171 = y170 + P0_2_3[0]; SB();
  const float y173 = y172 + P0_2_3[1]; SB();
  const float y174 = y173 + P0_3_3[0]; SB();
  WAITC(63, 11);
  const float y113 = y106 + i101; SB();
  WAITC(63, 10);
  const float y114 = y108 + i103; SB();
  WAITC(63, 9);
  const float y112 = P0_0_0[0] + i105; SB();
  const unsigned y116 = pk(y114, y111); SB();
  const unsigned y115 = pk(y112, y113); SB();
  LDS8(yw + 0) = v2u{y115, y116};
  WAITC(63, 9);
  const float y134 = y127 + i122; SB();
  WAITC(63, 8);
  const float y135 = y129 + i124; SB();
  WAITC(63, 7);
  const float y133 = P0_0_1[0] + i126; SB();
  const unsigned y137 = pk(y135, y132); SB();
  const unsigned y136 = pk(y133, y134); SB();
  LDS8(yw + 512) = v2u{y136, y137};
  WAITC(63, 7);
  const float y155 = y148 + i143; SB();
  WAITC(63, 6);
  const float y156 = y150 + i145; SB();
  WAITC(63, 5);
  const float y154 = P0_0_2[0] + i147; SB();
  const unsigned y158 = pk(y156, y153); SB();
  const unsigned y157 = pk(y154, y155); SB();
  LDS8(yw + 1024) = v2u{y157, y158};
  WAITC(63, 5);
  const float y176 = y169 + i164; SB();
  WAITC(63, 4);
  const float y177 = y171 + i166; SB();
  WAITC(63, 3);
  const float y175 = P0_0_3[0] + i168; SB();
  const unsigned y179 = pk(y177, y174); SB();
  const unsigned y178 = pk(y175, y176); SB();
  LDS8(yw + 1536) = v2u{y178, y179};
  WAITC(63, 0);
  BARRIER();
  const v2u yt181 = trd(yr + 0);
  const v2u yt186 = trd(yr + 16);
  const v2u yt191 = trd(yr + 256);
  const v2u yt196 = trd(yr + 272);
  WAITC(6, 3);
  const f32x2 u182a = {blo(qx0_0.x), bhi(qx0_0.x)}, u182 = PFMA(PFMA(u182a, (f32x2{__uint_as_float(qs0_0.x), __uint_as_float(qs0_0.x)}), (f32x2{__uint_as_float(qs0_0.y), __uint_as_float(qs0_0.y)})), (f32x2{blo(yt181.x), bhi(yt181.x)}), u182a); const unsigned x183 = pk(u182[0], u182[1]); SB();
  const f32x2 u184a = {blo(qx0_0.y), bhi(qx0_0.y)}, u184 = PFMA(PFMA(u184a, (f32x2{__uint_as_float(qs0_0.x), __uint_as_float(qs0_0.x)}), (f32x2{__uint_as_float(qs0_0.y), __uint_as_float(qs0_0.y)})), (f32x2{blo(yt181.y), bhi(yt181.y)}), u184a); const unsigned x185 = pk(u184[0], u184[1]); SB();
  WAITC(5, 2);
  const f32x2 u187a = {blo(qx0_1.x), bhi(qx0_1.x)}, u187 = PFMA(PFMA(u187a, (f32x2{__uint_as_float(qs0_1.x), __uint_as_float(qs0_1.x)}), (f32x2{__uint_as_float(qs0_1.y), __uint_as_float(qs0_1.y)})), (f32x2{blo(yt186.x), bhi(yt186.x)}), u187a); const unsigned x188 = pk(u187[0], u187[1]); SB();
  const f32x2 u189a = {blo(qx0_1.y), bhi(qx0_1.y)}, u189 = PFMA(PFMA(u189a, (f32x2{__uint_as_float(qs0_1.x), __uint_as_float(qs0_1.x)}), (f32x2{__uint_as_float(qs0_1.y), __uint_as_float(qs0_1.y)})), (f32x2{blo(yt186.y), bhi(yt186.y)}), u189a); const unsigned x190 = pk(u189[0], u189[1]); SB();
  WAITC(4, 1);
  const f32x2 u192a = {blo(qx0_2.x), bhi(qx0_2.x)}, u192 = PFMA(PFMA(u192a, (f32x2{__uint_as_float(qs0_2.x), __uint_as_float(qs0_2.x)}), (f32x2{__uint_as_float(qs0_2.y), __uint_as_float(qs0_2.y)})), (f32x2{blo(yt191.x), bhi(yt191.x)}), u192a); const unsigned x193 = pk(u192[0], u192[1]); SB();
  const f32x2 u194a = {blo(qx0_2.y), bhi(qx0_2.y)}, u194 = PFMA(PFMA(u194a, (f32x2{__uint_as_float(qs0_2.x), __uint_as_float(qs0_2.x)}), (f32x2{__uint_as_float(qs0_2.y), __uint_as_float(qs0_2.y)})), (f32x2{blo(yt191.y), bhi(yt191.y)}), u194a); const unsigned x195 = pk(u194[0], u194[1]); SB();
  WAITC(3, 0);
  const f32x2 u197a = {blo(qx0_3.x), bhi(qx0_3.x)}, u197 = PFMA(PFMA(u197a, (f32x2{__uint_as_float(qs0_3.x), __uint_as_float(qs0_3.x)}), (f32x2{__uint_as_float(qs0_3.y), __uint_as_float(qs0_3.y)})), (f32x2{blo(yt196.x), bhi(yt196.x)}), u197a); const unsigned x198 = pk(u197[0], u197[1]); SB();
  const v2u ba216 = LDS8(ub0 + 0); FENCE();
  const v2u bb217 = LDS8(ub1 + 0); FENCE();
  const v2u ba218 = LDS8(ub0 + 64); FENCE();
  const v2u bb219 = LDS8(ub1 + 64); FENCE();
  const f32x2 u199a = {blo(qx0_3.y), bhi(qx0_3.y)}, u199 = PFMA(PFMA(u199a, (f32x2{__uint_as_float(qs0_3.x), __uint_as_float(qs0_3.x)}), (f32x2{__uint_as_float(qs0_3.y), __uint_as_float(qs0_3.y)})), (f32x2{blo(yt196.y), bhi(yt196.y)}), u199a); const unsigned x200 = pk(u199[0], u199[1]); SB();
  int so201; SADD(so201, x1b, 0);
  st16(v4u{x183, x185, x188, x190}, ws, vqz, so201);
  int so202; SADD(so202, yb, 0);
  st16(cat(yt181, yt186), ws, vqz, so202);
  int so203; SADD(so203, x1b, 163840);
  st16(v4u{x193, x195, x198, x200}, ws, vqz, so203);
  int so204; SADD(so204, yb, 163840);
  st16(cat(yt191, yt196), ws, vqz, so204);
  STAMP(6);
  WAITC(63, 2);
  f32x4 P4_0_0; MFA0(P4_0_0, A9, cat(ba216, bb217));
  const v2u ba220 = LDS8(ub0 + 128); FENCE();
  const v2u bb221 = LDS8(ub1 + 128); FENCE();
  f32x4 P4_0_1; MFA0(P4_0_1, A11, cat(ba216, bb217));
  DMA(so4, 3072);
  int so5; SADD(so5, so4, 4096);
  f32x4 P4_0_2; MFA0(P4_0_2, A13, cat(ba216, bb217));
  SETM0(w1d, 4096);
  f32x4 P4_0_3; MFA0(P4_0_3, A15, cat(ba216, bb217));
  WAITC(63, 2);
  MFA(P4_0_0, A8, cat(ba218, bb219));
  const v2u ba222 = LDS8(ub0 + 192); FENCE();
  const v2u bb223 = LDS8(ub1 + 192); FENCE();
  MFA(P4_0_1, A10, cat(ba218, bb219));
  DMA(so5, 0);
  MFA(P4_0_2, A12, cat(ba218, bb219));
  MFA(P4_0_3, A14, cat(ba218, bb219));
  WAITC(63, 2);
  MFA(P4_0_0, A7, cat(ba220, bb221));
  const v2u ba224 = LDS8(ub0 + 256); FENCE();
  const v2u bb225 = LDS8(ub1 + 256); FENCE();
  MFA(P4_0_1, A9, cat(ba220, bb221));
  DMA(so5, 1024);
  MFA(P4_0_2, A11, cat(ba220, bb221));
  MFA(P4_0_3, A13, cat(ba220, bb221));
  WAITC(63, 2);
  MFA(P4_0_0, A6, cat(ba222, bb223));
  const v2u ba226 = LDS8(ub0 + 320); FENCE();
  const v2u bb227 = LDS8(ub1 + 320); FENCE();
  MFA(P4_0_1, A8, cat(ba222, bb223));
  DMA(so5, 2048);
  int so8; SMUL(so8, xo1, 7);
  MFA(P4_0_2, A10, cat(ba222, bb223));
  int so9; SADDS(so9, w1s, so8);
  SETM0(w1d, 28672);
  MFA(P4_0_3, A12, cat(ba222, bb223));
  WAITC(63, 2);
  MFA(P4_0_0, A5, cat(ba224, bb225));
  const v2u ba228 = LDS8(ub0 + 384); FENCE();
  const v2u bb229 = LDS8(ub1 + 384); FENCE();
  MFA(P4_0_1, A7, cat(ba224, bb225));
  DMA(so9, 0);
  MFA(P4_0_2, A9, cat(ba224, bb225));
  MFA(P4_0_3, A11, cat(ba224, bb225));
  WAITC(63, 2);
  MFA(P4_0_0, A4, cat(ba226, bb227));
  const v2u ba230 = LDS8(ub0 + 448); FENCE();
  const v2u bb231 = LDS8(ub1 + 448); FENCE();
  MFA(P4_0_1, A6, cat(ba226, bb227));
  POLL_LD(pl206, vp8, (C_S0V) * 256);
  MFA(P4_0_2, A8, cat(ba226, bb227));
  MFA(P4_0_3, A10, cat(ba226, bb227));
  WAITC(63, 2);
  MFA(P4_0_0, A3, cat(ba228, bb229));
  const v2u ba232 = LDS8(ub0 + 512); FENCE();
  const v2u bb233 = LDS8(ub1 + 512); FENCE();
  MFA(P4_0_1, A5, cat(ba228, bb229));
  DMA(so9, 1024);
  MFA(P4_0_2, A7, cat(ba228, bb229));
  MFA(P4_0_3, A9, cat(ba228, bb229));
  WAITC(63, 2);
  MFA(P4_0_0, A2, cat(ba230, bb231));
  const v2u ba234 = LDS8(ub0 + 576); FENCE();
  const v2u bb235 = LDS8(ub1 + 576); FENCE();
  MFA(P4_0_1, A4, cat(ba230, bb231));
  DMA(so9, 2048);
  MFA(P4_0_2, A6, cat(ba230, bb231));
  MFA(P4_0_3, A8, cat(ba230, bb231));
  WAITC(63, 2);
  MFA(P4_0_0, A1, cat(ba232, bb233));
  const v2u ba236 = LDS8(ub0 + 640); FENCE();
  const v2u bb237 = LDS8(ub1 + 640); FENCE();
  MFA(P4_0_1, A3, cat(ba232, bb233));
  DMA(so9, 3072);
  int so10; SADD(so10, so9, 4096);
  MFA(P4_0_2, A5, cat(ba232, bb233));
  SETM0(w1d, 32768);
  MFA(P4_0_3, A7, cat(ba232, bb233));
  WAITC(63, 2);
  const v2u ba238 = LDS8(ub0 + 704); FENCE();
  const v2u bb239 = LDS8(ub1 + 704); FENCE();
  MFA(P4_0_0, A0, cat(ba234, bb235));
  MFA(P4_0_1, A2, cat(ba234, bb235));
  DMA(so10, 0);
  MFA(P4_0_2, A4, cat(ba234, bb235));
  const v2u ba240 = LDS8(ub0 + 768); FENCE();
  const v2u bb241 = LDS8(ub1 + 768); FENCE();
  MFA(P4_0_3, A6, cat(ba234, bb235));
  WAITC(63, 4);
  MFA(P4_0_1, A1, cat(ba236, bb237));
  const v2u ba242 = LDS8(ub0 + 832); FENCE();
  const v2u bb243 = LDS8(ub1 + 832); FENCE();
  MFA(P4_0_2, A3, cat(ba236, bb237));
  DMA(so10, 1024);
  MFA(P4_0_3, A5, cat(ba236, bb237));
  WAITC(63, 4);
  const v2u ba244 = LDS8(ub0 + 896); FENCE();
  const v2u bb245 = LDS8(ub1 + 896); FENCE();
  MFA(P4_0_1, A0, cat(ba238, bb239));
  const v2u ba246 = LDS8(ub0 + 960); FENCE();
  const v2u bb247 = LDS8(ub1 + 960); FENCE();
  MFA(P4_0_2, A2, cat(ba238, bb239));
  const v2u ba248 = LDS8(ub0 + 1152); FENCE();
  const v2u bb249 = LDS8(ub1 + 1152); FENCE();
  MFA(P4_0_3, A4, cat(ba238, bb239));
  WAITC(63, 8);
  DMA(so10, 2048);
  int so13; SMUL(so13, xo2, 7);
  MFA(P4_0_2, A1, cat(ba240, bb241));
  int so14; SADDS(so14, w1s, so13);
  SETM0(w1d, 57344);
  MFA(P4_0_3, A3, cat(ba240, bb241));
  WAITC(63, 6);
  MFA(P4_0_2, A0, cat(ba242, bb243));
  const v2u ba250 = LDS8(ub0 + 1216); FENCE();
  const v2u bb251 = LDS8(ub1 + 1216); FENCE();
  MFA(P4_0_3, A2, cat(ba242, bb243));
  WAITC(63, 6);
  DMA(so14, 0);
  MFA(P4_0_3, A1, cat(ba244, bb245));
  WAITC(63, 4);
  MFA(P4_0_3, A0, cat(ba246, bb247));
  WAITC(63, 2);
  f32x4 P4_1_0; MFA0(P4_1_0, A9, cat(ba248, bb249));
  const v2u ba252 = LDS8(ub0 + 1280); FENCE();
  const v2u bb253 = LDS8(ub1 + 1280); FENCE();
  f32x4 P4_1_1; MFA0(P4_1_1, A11, cat(ba248, bb249));
  DMA(so14, 1024);
  f32x4 P4_1_2; MFA0(P4_1_2, A13, cat(ba248, bb249));
  f32x4 P4_1_3; MFA0(P4_1_3, A15, cat(ba248, bb249));
  WAITC(63, 2);
  MFA(P4_1_0, A8, cat(ba250, bb251));
  const v2u ba254 = LDS8(ub0 + 1344); FENCE();
  const v2u bb255 = LDS8(ub1 + 1344); FENCE();
  MFA(P4_1_1, A10, cat(ba250, bb251));
  MFA(P4_1_2, A12, cat(ba250, bb251));
  WAITC(8, 15);
  POLL_CKN(pl206, vp8, (C_S0V) * 256, 12);
  MFA(P4_1_3, A14, cat(ba250, bb251));
  WAITC(63, 2);
  DRAINBAR(14);
  MFA(P4_1_0, A7, cat(ba252, bb253));
  const v2u ba256 = LDS8(ub0 + 1408); FENCE();
  const v2u bb257 = LDS8(ub1 + 1408); FENCE();
  MFA(P4_1_1, A9, cat(ba252, bb253));
  ARRIVE(va8, (C_S1A) * 256);
  MFA(P4_1_2, A11, cat(ba252, bb253));
  MFA(P4_1_3, A13, cat(ba252, bb253));
  WAITC(63, 2);
  MFA(P4_1_0, A6, cat(ba254, bb255));
  const v2u ba258 = LDS8(ub0 + 1472); FENCE();
  const v2u bb259 = LDS8(ub1 + 1472); FENCE();
  MFA(P4_1_1, A8, cat(ba254, bb255));
  DMA(so14, 2048);
  MFA(P4_1_2, A10, cat(ba254, bb255));
  MFA(P4_1_3, A12, cat(ba254, bb255));
  WAITC(63, 2);
  MFA(P4_1_0, A5, cat(ba256, bb257));
  const v2u ba260 = LDS8(ub0 + 1536); FENCE();
  const v2u bb261 = LDS8(ub1 + 1536); FENCE();
  MFA(P4_1_1, A7, cat(ba256, bb257));
  DMA(so14, 3072);
  int so15; SADD(so15, so14, 4096);
  MFA(P4_1_2, A9, cat(ba256, bb257));
  SETM0(w1d, 61440);
  MFA(P4_1_3, A11, cat(ba256, bb257));
  WAITC(63, 2);
  MFA(P4_1_0, A4, cat(ba258, bb259));
  const v2u ba262 = LDS8(ub0 + 1600); FENCE();
  const v2u bb263 = LDS8(ub1 + 1600); FENCE();
  MFA(P4_1_1, A6, cat(ba258, bb259));
  DMA(so15, 0);
  MFA(P4_1_2, A8, cat(ba258, bb259));
  MFA(P4_1_3, A10, cat(ba258, bb259));
  WAITC(63, 2);
  MFA(P4_1_0, A3, cat(ba260, bb261));
  const v2u ba264 = LDS8(ub0 + 1664); FENCE();
  const v2u bb265 = LDS8(ub1 + 1664); FENCE();
  MFA(P4_1_1, A5, cat(ba260, bb261));
  DMA(so15, 1024);
  MFA(P4_1_2, A7, cat(ba260, bb261));
  MFA(P4_1_3, A9, cat(ba260, bb261));
  WAITC(63, 2);
  MFA(P4_1_0, A2, cat(ba262, bb263));
  const v2u ba266 = LDS8(ub0 + 1728); FENCE();
  const v2u bb267 = LDS8(ub1 + 1728); FENCE();
  MFA(P4_1_1, A4, cat(ba262, bb263));
  DMA(so15, 2048);
  int so18; SMUL(so18, xo3, 7);
  MFA(P4_1_2, A6, cat(ba262, bb263));
  int so19; SADDS(so19, w1s, so18);
  SETM0(w1d, 86016);
  MFA(P4_1_3, A8, cat(ba262, bb263));
  WAITC(63, 2);
  MFA(P4_1_0, A1, cat(ba264, bb265));
  const v2u ba268 = LDS8(ub0 + 1792); FENCE();
  const v2u bb269 = LDS8(ub1 + 1792); FENCE();
  MFA(P4_1_1, A3, cat(ba264, bb265));
  DMA(so19, 0);
  MFA(P4_1_2, A5, cat(ba264, bb265));
  MFA(P4_1_3, A7, cat(ba264, bb265));
  WAITC(63, 2);
  const v2u ba270 = LDS8(ub0 + 1856); FENCE();
  const v2u bb271 = LDS8(ub1 + 1856); FENCE();
  MFA(P4_1_0, A0, cat(ba266, bb267));
  MFA(P4_1_1, A2, cat(ba266, bb267));
  DMA(so19, 1024);
  MFA(P4_1_2, A4, cat(ba266, bb267));
  const v2u ba272 = LDS8(ub0 + 1920); FENCE();
  const v2u bb273 = LDS8(ub1 + 1920); FENCE();
  MFA(P4_1_3, A6, cat(ba266, bb267));
  WAITC(63, 4);
  MFA(P4_1_1, A1, cat(ba268, bb269));
  const v2u ba274 = LDS8(ub0 + 1984); FENCE();
  const v2u bb275 = LDS8(ub1 + 1984); FENCE();
  MFA(P4_1_2, A3, cat(ba268, bb269));
  DMA(so19, 2048);
  MFA(P4_1_3, A5, cat(ba268, bb269));
  WAITC(63, 4);
  const v2u ba276 = LDS8(ub0 + 2048); FENCE();
  const v2u bb277 = LDS8(ub1 + 2048); FENCE();
  MFA(P4_1_1, A0, cat(ba270, bb271));
  const v2u ba278 = LDS8(ub0 + 2112); FENCE();
  const v2u bb279 = LDS8(ub1 + 2112); FENCE();
  MFA(P4_1_2, A2, cat(ba270, bb271));
  const v2u ba280 = LDS8(ub0 + 2304); FENCE();
  const v2u bb281 = LDS8(ub1 + 2304); FENCE();
  MFA(P4_1_3, A4, cat(ba270, bb271));
  WAITC(63, 8);
  DMA(so19, 3072);
  int so20; SADD(so20, so19, 4096);
  MFA(P4_1_2, A1, cat(ba272, bb273));
  SETM0(w1d, 90112);
  MFA(P4_1_3, A3, cat(ba272, bb273));
  WAITC(63, 6);
  MFA(P4_1_2, A0, cat(ba274, bb275));
  const v2u ba282 = LDS8(ub0 + 2368); FENCE();
  const v2u bb283 = LDS8(ub1 + 2368); FENCE();
  MFA(P4_1_3, A2, cat(ba274, bb275));
  WAITC(63, 6);
  DMA(so20, 0);
  MFA(P4_1_3, A1, cat(ba276, bb277));
  WAITC(63, 4);
  MFA(P4_1_3, A0, cat(ba278, bb279));
  WAITC(63, 2);
  f32x4 P4_2_0; MFA0(P4_2_0, A9, cat(ba280, bb281));
  const v2u ba284 = LDS8(ub0 + 2432); FENCE();
  const v2u bb285 = LDS8(ub1 + 2432); FENCE();
  f32x4 P4_2_1; MFA0(P4_2_1, A11, cat(ba280, bb281));
  DMA(so20, 1024);
  f32x4 P4_2_2; MFA0(P4_2_2, A13, cat(ba280, bb281));
  f32x4 P4_2_3; MFA0(P4_2_3, A15, cat(ba280, bb281));
  WAITC(63, 2);
  MFA(P4_2_0, A8, cat(ba282, bb283));
  const v2u ba286 = LDS8(ub0 + 2496); FENCE();
  const v2u bb287 = LDS8(ub1 + 2496); FENCE();
  MFA(P4_2_1, A10, cat(ba282, bb283));
  DMA(so20, 2048);
  int so23; SMUL(so23, xo4, 7);
  MFA(P4_2_2, A12, cat(ba282, bb283));
  int so24; SADDS(so24, w1s, so23);
  SETM0(w1d, 114688);
  MFA(P4_2_3, A14, cat(ba282, bb283));
  WAITC(63, 2);
  MFA(P4_2_0, A7, cat(ba284, bb285));
  const v2u ba288 = LDS8(ub0 + 2560); FENCE();
  const v2u bb289 = LDS8(ub1 + 2560); FENCE();
  MFA(P4_2_1, A9, cat(ba284, bb285));
  DMA(so24, 0);
  MFA(P4_2_2, A11, cat(ba284, bb285));
  MFA(P4_2_3, A13, cat(ba284, bb285));
  WAITC(63, 2);
  MFA(P4_2_0, A6, cat(ba286, bb287));
  const v2u ba290 = LDS8(ub0 + 2624); FENCE();
  const v2u bb291 = LDS8(ub1 + 2624); FENCE();
  MFA(P4_2_1, A8, cat(ba286, bb287));
  DMA(so24, 1024);
  MFA(P4_2_2, A10, cat(ba286, bb287));
  MFA(P4_2_3, A12, cat(ba286, bb287));
  WAITC(63, 2);
  MFA(P4_2_0, A5, cat(ba288, bb289));
  const v2u ba292 = LDS8(ub0 + 2688); FENCE();
  const v2u bb293 = LDS8(ub1 + 2688); FENCE();
  MFA(P4_2_1, A7, cat(ba288, bb289));
  DMA(so24, 2048);
  MFA(P4_2_2, A9, cat(ba288, bb289));
  MFA(P4_2_3, A11, cat(ba288, bb289));
  WAITC(63, 2);
  MFA(P4_2_0, A4, cat(ba290, bb291));
  const v2u ba294 = LDS8(ub0 + 2752); FENCE();
  const v2u bb295 = LDS8(ub1 + 2752); FENCE();
  MFA(P4_2_1, A6, cat(ba290, bb291));
  DMA(so24, 3072);
  int so25; SADD(so25, so24, 4096);
  MFA(P4_2_2, A8, cat(ba290, bb291));
  SETM0(w1d, 118784);
  MFA(P4_2_3, A10, cat(ba290, bb291));
  WAITC(63, 2);
  MFA(P4_2_0, A3, cat(ba292, bb293));
  const v2u ba296 = LDS8(ub0 + 2816); FENCE();
  const v2u bb297 = LDS8(ub1 + 2816); FENCE();
  MFA(P4_2_1, A5, cat(ba292, bb293));
  DMA(so25, 0);
  MFA(P4_2_2, A7, cat(ba292, bb293));
  MFA(P4_2_3, A9, cat(ba292, bb293));
  WAITC(63, 2);
  MFA(P4_2_0, A2, cat(ba294, bb295));
  const v2u ba298 = LDS8(ub0 + 2880); FENCE();
  const v2u bb299 = LDS8(ub1 + 2880); FENCE();
  MFA(P4_2_1, A4, cat(ba294, bb295));
  DMA(so25, 1024);
  MFA(P4_2_2, A6, cat(ba294, bb295));
  MFA(P4_2_3, A8, cat(ba294, bb295));
  WAITC(63, 2);
  MFA(P4_2_0, A1, cat(ba296, bb297));
  const v2u ba300 = LDS8(ub0 + 2944); FENCE();
  const v2u bb301 = LDS8(ub1 + 2944); FENCE();
  MFA(P4_2_1, A3, cat(ba296, bb297));
  DMA(so25, 2048);
  int so211; SADD(so211, qzb, 1310720);
  MFA(P4_2_2, A5, cat(ba296, bb297));
  MFA(P4_2_3, A7, cat(ba296, bb297));
  WAITC(63, 2);
  const v2u ba302 = LDS8(ub0 + 3008); FENCE();
  const v2u bb303 = LDS8(ub1 + 3008); FENCE();
  MFA(P4_2_0, A0, cat(ba298, bb299));
  MFA(P4_2_1, A2, cat(ba298, bb299));
  const v2u qx1_0 = ld8(ws, vqz, so211, SC1);
  int so212; SADD(so212, qzb, 1320960);
  MFA(P4_2_2, A4, cat(ba298, bb299));
  const v2u ba304 = LDS8(ub0 + 3072); FENCE();
  const v2u bb305 = LDS8(ub1 + 3072); FENCE();
  MFA(P4_2_3, A6, cat(ba298, bb299));
  WAITC(63, 4);
  MFA(P4_2_1, A1, cat(ba300, bb301));
  const v2u ba306 = LDS8(ub0 + 3136); FENCE();
  const v2u bb307 = LDS8(ub1 + 3136); FENCE();
  MFA(P4_2_2, A3, cat(ba300, bb301));
  const v2u qx1_1 = ld8(ws, vqz, so212, SC1);
  int so213; SADD(so213, qzb, 1474560);
  MFA(P4_2_3, A5, cat(ba300, bb301));
  WAITC(63, 4);
  const v2u ba308 = LDS8(ub0 + 3200); FENCE();
  const v2u bb309 = LDS8(ub1 + 3200); FENCE();
  MFA(P4_2_1, A0, cat(ba302, bb303));
  const v2u ba310 = LDS8(ub0 + 3264); FENCE();
  const v2u bb311 = LDS8(ub1 + 3264); FENCE();
  MFA(P4_2_2, A2, cat(ba302, bb303));
  const v2u ba312 = LDS8(ub0 + 3456); FENCE();
  const v2u bb313 = LDS8(ub1 + 3456); FENCE();
  MFA(P4_2_3, A4, cat(ba302, bb303));
  WAITC(63, 8);
  const v2u qx1_2 = ld8(ws, vqz, so213, SC1);
  int so214; SADD(so214, qzb, 1484800);
  MFA(P4_2_2, A1, cat(ba304, bb305));
  MFA(P4_2_3, A3, cat(ba304, bb305));
  WAITC(63, 6);
  MFA(P4_2_2, A0, cat(ba306, bb307));
  const v2u ba314 = LDS8(ub0 + 3520); FENCE();
  const v2u bb315 = LDS8(ub1 + 3520); FENCE();
  MFA(P4_2_3, A2, cat(ba306, bb307));
  WAITC(63, 6);
  const v2u qx1_3 = ld8(ws, vqz, so214, SC1);
  int so215; SADD(so215, stb, 16384);
  MFA(P4_2_3, A1, cat(ba308, bb309));
  WAITC(63, 4);
  MFA(P4_2_3, A0, cat(ba310, bb311));
  WAITC(63, 2);
  f32x4 P4_3_0; MFA0(P4_3_0, A9, cat(ba312, bb313));
  const v2u ba316 = LDS8(ub0 + 3584); FENCE();
  const v2u bb317 = LDS8(ub1 + 3584); FENCE();
  f32x4 P4_3_1; MFA0(P4_3_1, A11, cat(ba312, bb313));
  const v2u qs1_0 = ld8(ws, vst0 + 0, so215, SC1);
  f32x4 P4_3_2; MFA0(P4_3_2, A13, cat(ba312, bb313));
  f32x4 P4_3_3; MFA0(P4_3_3, A15, cat(ba312, bb313));
  WAITC(63, 2);
  MFA(P4_3_0, A8, cat(ba314, bb315));
  const v2u ba318 = LDS8(ub0 + 3648); FENCE();
  const v2u bb319 = LDS8(ub1 + 3648); FENCE();
  MFA(P4_3_1, A10, cat(ba314, bb315));
  POLL_LD(pl205, vp8, (C_S1A) * 256);
  MFA(P4_3_2, A12, cat(ba314, bb315));
  MFA(P4_3_3, A14, cat(ba314, bb315));
  WAITC(63, 2);
  MFA(P4_3_0, A7, cat(ba316, bb317));
  const v2u ba320 = LDS8(ub0 + 3712); FENCE();
  const v2u bb321 = LDS8(ub1 + 3712); FENCE();
  MFA(P4_3_1, A9, cat(ba316, bb317));
  const v2u qs1_1 = ld8(ws, vst0 + 128, so215, SC1);
  MFA(P4_3_2, A11, cat(ba316, bb317));
  MFA(P4_3_3, A13, cat(ba316, bb317));
  WAITC(63, 2);
  MFA(P4_3_0, A6, cat(ba318, bb319));
  const v2u ba322 = LDS8(ub0 + 3776); FENCE();
  const v2u bb323 = LDS8(ub1 + 3776); FENCE();
  MFA(P4_3_1, A8, cat(ba318, bb319));
  const v2u qs1_2 = ld8(ws, vst0 + 2048, so215, SC1);
  MFA(P4_3_2, A10, cat(ba318, bb319));
  MFA(P4_3_3, A12, cat(ba318, bb319));
  WAITC(63, 2);
  MFA(P4_3_0, A5, cat(ba320, bb321));
  const v2u ba324 = LDS8(ub0 + 3840); FENCE();
  const v2u bb325 = LDS8(ub1 + 3840); FENCE();
  MFA(P4_3_1, A7, cat(ba320, bb321));
  const v2u qs1_3 = ld8(ws, vst0 + 2176, so215, SC1);
  int so207; SMUL(so207, ko0, 5);
  MFA(P4_3_2, A9, cat(ba320, bb321));
  int so208; SADDS(so208, w2p, so207);
  MFA(P4_3_3, A11, cat(ba320, bb321));
  WAITC(63, 2);
  MFA(P4_3_0, A4, cat(ba322, bb323));
  const v2u ba326 = LDS8(ub0 + 3904); FENCE();
  const v2u bb327 = LDS8(ub1 + 3904); FENCE();
  MFA(P4_3_1, A6, cat(ba322, bb323));
  W0 = ld16(ws, vo0 + 0, so208, SC1);
  MFA(P4_3_2, A8, cat(ba322, bb323));
  MFA(P4_3_3, A10, cat(ba322, bb323));
  WAITC(63, 2);
  MFA(P4_3_0, A3, cat(ba324, bb325));
  const v2u ba328 = LDS8(ub0 + 3968); FENCE();
  const v2u bb329 = LDS8(ub1 + 3968); FENCE();
  MFA(P4_3_1, A5, cat(ba324, bb325));
  W1 = ld16(ws, vo0 + 1024, so208, SC1);
  int so209; SADDS(so209, w2q, so207);
  MFA(P4_3_2, A7, cat(ba324, bb325));
  MFA(P4_3_3, A9, cat(ba324, bb325));
  WAITC(63, 2);
  MFA(P4_3_0, A2, cat(ba326, bb327));
  const v2u ba330 = LDS8(ub0 + 4032); FENCE();
  const v2u bb331 = LDS8(ub1 + 4032); FENCE();
  MFA(P4_3_1, A4, cat(ba326, bb327));
  W2 = ld16(ws, vo0 + 0, so209, SC1);
  MFA(P4_3_2, A6, cat(ba326, bb327));
  MFA(P4_3_3, A8, cat(ba326, bb327));
  WAITC(63, 2);
  MFA(P4_3_0, A1, cat(ba328, bb329));
  const v2u ba332 = LDS8(ub0 + 4096); FENCE();
  const v2u bb333 = LDS8(ub1 + 4096); FENCE();
  MFA(P4_3_1, A3, cat(ba328, bb329));
  W3 = ld16(ws, vo0 + 1024, so209, SC1);
  MFA(P4_3_2, A5, cat(ba328, bb329));
  MFA(P4_3_3, A7, cat(ba328, bb329));
  WAITC(63, 2);
  const v2u ba334 = LDS8(ub0 + 4160); FENCE();
  const v2u bb335 = LDS8(ub1 + 4160); FENCE();
  MFA(P4_3_0, A0, cat(ba330, bb331));
  WAITC(7, 15);
  POLL_CK(pl205, vp8, (C_S1A) * 256, 24);
  MFA(P4_3_1, A2, cat(ba330, bb331));
  cbv = CB_LOAD();
  int so210; SADDS(so210, xb00, xo0);
  MFA(P4_3_2, A4, cat(ba330, bb331));
  const v2u ba336 = LDS8(ub0 + 4224); FENCE();
  const v2u bb337 = LDS8(ub1 + 4224); FENCE();
  MFA(P4_3_3, A6, cat(ba330, bb331));
  WAITC(63, 4);
  MFA(P4_3_1, A1, cat(ba332, bb333));
  const v2u ba338 = LDS8(ub0 + 4288); FENCE();
  const v2u bb339 = LDS8(ub1 + 4288); FENCE();
  MFA(P4_3_2, A3, cat(ba332, bb333));
  x00_0 = ld16(ws, vo0 + 0, so210, SC1);
  MFA(P4_3_3, A5, cat(ba332, bb333));
  WAITC(63, 4);
  const v2u ba340 = LDS8(ub0 + 4352); FENCE();
  const v2u bb341 = LDS8(ub1 + 4352); FENCE();
  MFA(P4_3_1, A0, cat(ba334, bb335));
  const v2u ba342 = LDS8(ub0 + 4416); FENCE();
  const v2u bb343 = LDS8(ub1 + 4416); FENCE();
  MFA(P4_3_2, A2, cat(ba334, bb335));
  MFA(P4_3_3, A4, cat(ba334, bb335));
  WAITC(63, 6);
  x00_1 = ld16(ws, vo0 + 1024, so210, SC1);
  MFA(P4_3_2, A1, cat(ba336, bb337));
  MFA(P4_3_3, A3, cat(ba336, bb337));
  WAITC(63, 4);
  MFA(P4_3_2, A0, cat(ba338, bb339));
  MFA(P4_3_3, A2, cat(ba338, bb339));
  WAITC(63, 2);
  x00_2 = ld16(ws, vo0 + 2048, so210, SC1);
  MFA(P4_3_3, A1, cat(ba340, bb341));
  WAITC(63, 0);
  MFA(P4_3_3, A0, cat(ba342, bb343));
  STAMP(7);
  NOP8(); TIE(P4_0_0); TIE(P4_0_1); TIE(P4_0_2); TIE(P4_0_3); TIE(P4_1_0); TIE(P4_1_1); TIE(P4_1_2); TIE(P4_1_3); TIE(P4_2_0); TIE(P4_2_1); TIE(P4_2_2); TIE(P4_2_3); TIE(P4_3_0); TIE(P4_3_1); TIE(P4_3_2); TIE(P4_3_3);
  const float c344 = P4_1_0[3] + P4_2_0[2]; SB();
  const float c346 = P4_2_0[3] + P4_3_0[2]; SB();
  const float c345 = c344 + P4_3_0[1]; SB();
  const float s347 = top ? c162 : c346; SB();
  const float i348 = __int_as_float(__builtin_amdgcn_ds_bpermute(bsrc, __float_as_int(s347)));
  const float s349 = top ? P0_3_3[3] : P4_3_0[3]; SB();
  const float i350 = __int_as_float(__builtin_amdgcn_ds_bpermute(bsrc, __float_as_int(s349)));
  const float s351 = top ? c161 : c345; SB();
  const float i352 = __int_as_float(__builtin_amdgcn_ds_bpermute(bsrc, __float_as_int(s351)));
  const float c365 = P4_1_1[3] + P4_2_1[2]; SB();
  const float c367 = P4_2_1[3] + P4_3_1[2]; SB();
  const float c366 = c365 + P4_3_1[1]; SB();
  const float s368 = top ? c346 : c367; SB();
  int so428; SADDS(so428, xb00, xo0);
  x00_3 = ld16(ws, vo0 + 3072, so428, SC1);
  const float i369 = __int_as_float(__builtin_amdgcn_ds_bpermute(bsrc, __float_as_int(s368)));
  const float s370 = top ? P4_3_0[3] : P4_3_1[3]; SB();
  const float i371 = __int_as_float(__builtin_amdgcn_ds_bpermute(bsrc, __float_as_int(s370)));
  const float s372 = top ? c345 : c366; SB();
  const float i373 = __int_as_float(__builtin_amdgcn_ds_bpermute(bsrc, __float_as_int(s372)));
  const float c386 = P4_1_2[3] + P4_2_2[2]; SB();
  const float c388 = P4_2_2[3] + P4_3_2[2]; SB();
  const float c387 = c386 + P4_3_2[1]; SB();
  const float s389 = top ? c367 : c388; SB();
  const float i390 = __int_as_float(__builtin_amdgcn_ds_bpermute(bsrc, __float_as_int(s389)));
  const float s391 = top ? P4_3_1[3] : P4_3_2[3]; SB();
  const float i392 = __int_as_float(__builtin_amdgcn_ds_bpermute(bsrc, __float_as_int(s391)));
  int so429; SADDS(so429, xb00, xo1);
  x00_4 = ld16(ws, vo0 + 0, so429, SC1);
  const float s393 = top ? c366 : c387; SB();
  const float i394 = __int_as_float(__builtin_amdgcn_ds_bpermute(bsrc, __float_as_int(s393)));
  const float c407 = P4_1_3[3] + P4_2_3[2]; SB();
  const float c409 = P4_2_3[3] + P4_3_3[2]; SB();
  const float c408 = c407 + P4_3_3[1]; SB();
  const float s410 = top ? c388 : c409; SB();
  const float i411 = __int_as_float(__builtin_amdgcn_ds_bpermute(bsrc, __float_as_int(s410)));
  const float s412 = top ? P4_3_2[3] : P4_3_3[3]; SB();
  const float i413 = __int_as_float(__builtin_amdgcn_ds_bpermute(bsrc, __float_as_int(s412)));
  const float s414 = top ? c387 : c408; SB();
  const float i415 = __int_as_float(__builtin_amdgcn_ds_bpermute(bsrc, __float_as_int(s414)));
  const float y353 = P4_0_0[1] + P4_1_0[0]; SB();
  x00_5 = ld16(ws, vo0 + 1024, so429, SC1);
  const float y354 = P4_0_0[2] + P4_1_0[1]; SB();
  const float y356 = P4_0_0[3] + P4_1_0[2]; SB();
  const float y355 = y354 + P4_2_0[0]; SB();
  const float y357 = y356 + P4_2_0[1]; SB();
  const float y358 = y357 + P4_3_0[0]; SB();
  const float y374 = P4_0_1[1] + P4_1_1[0]; SB();
  const float y375 = P4_0_1[2] + P4_1_1[1]; SB();
  const float y377 = P4_0_1[3] + P4_1_1[2]; SB();
  const float y376 = y375 + P4_2_1[0]; SB();
  const float y378 = y377 + P4_2_1[1]; SB();
  const float y379 = y378 + P4_3_1[0]; SB();
  const float y395 = P4_0_2[1] + P4_1_2[0]; SB();
  const float y396 = P4_0_2[2] + P4_1_2[1]; SB();
  const float y398 = P4_0_2[3] + P4_1_2[2]; SB();
  const float y397 = y396 + P4_2_2[0]; SB();
  const float y399 = y398 + P4_2_2[1]; SB();
  x00_6 = ld16(ws, vo0 + 2048, so429, SC1);
  const float y400 = y399 + P4_3_2[0]; SB();
  const float y416 = P4_0_3[1] + P4_1_3[0]; SB();
  const float y417 = P4_0_3[2] + P4_1_3[1]; SB();
  const float y419 = P4_0_3[3] + P4_1_3[2]; SB();
  const float y418 = y417 + P4_2_3[0]; SB();
  const float y420 = y419 + P4_2_3[1]; SB();
  const float y421 = y420 + P4_3_3[0]; SB();
  WAITC(63, 11);
  const float y360 = y353 + i348; SB();
  WAITC(63, 10);
  const float y361 = y355 + i350; SB();
  WAITC(63, 9);
  const float y359 = P4_0_0[0] + i352; SB();
  const unsigned y363 = pk(y361, y358); SB();
  const unsigned y362 = pk(y359, y360); SB();
  LDS8(ywb + 0) = v2u{y362, y363};
  WAITC(63, 9);
  const float y381 = y374 + i369; SB();
  x00_7 = ld16(ws, vo0 + 3072, so429, SC1);
  WAITC(63, 8);
  const float y382 = y376 + i371; SB();
  WAITC(63, 7);
  const float y380 = P4_0_1[0] + i373; SB();
  const unsigned y384 = pk(y382, y379); SB();
  const unsigned y383 = pk(y380, y381); SB();
  LDS8(ywb + 512) = v2u{y383, y384};
  WAITC(63, 7);
  const float y402 = y395 + i390; SB();
  WAITC(63, 6);
  const float y403 = y397 + i392; SB();
  WAITC(63, 5);
  const float y401 = P4_0_2[0] + i394; SB();
  const unsigned y405 = pk(y403, y400); SB();
  const unsigned y404 = pk(y401, y402); SB();
  LDS8(ywb + 1024) = v2u{y404, y405};
  WAITC(63, 5);
  const float y423 = y416 + i411; SB();
  WAITC(63, 4);
  const float y424 = y418 + i413; SB();
  WAITC(63, 3);
  const float y422 = P4_0_3[0] + i415; SB();
  const unsigned y426 = pk(y424, y421); SB();
  const unsigned y425 = pk(y422, y423); SB();
  LDS8(ywb + 1536) = v2u{y425, y426};
  WAITC(22, 0); BARRIER();
  const v2u yt430 = trd(yrb + 0);
  const v2u yt435 = trd(yrb + 16);
  const v2u yt440 = trd(yrb + 256);
  const v2u yt445 = trd(yrb + 272);
  WAITC(63, 3);
  const f32x2 u431a = {blo(qx1_0.x), bhi(qx1_0.x)}, u431 = PFMA(PFMA(u431a, (f32x2{__uint_as_float(qs1_0.x), __uint_as_float(qs1_0.x)}), (f32x2{__uint_as_float(qs1_0.y), __uint_as_float(qs1_0.y)})), (f32x2{blo(yt430.x), bhi(yt430.x)}), u431a); const unsigned x432 = pk(u431[0], u431[1]); SB();
  const f32x2 u433a = {blo(qx1_0.y), bhi(qx1_0.y)}, u433 = PFMA(PFMA(u433a, (f32x2{__uint_as_float(qs1_0.x), __uint_as_float(qs1_0.x)}), (f32x2{__uint_as_float(qs1_0.y), __uint_as_float(qs1_0.y)})), (f32x2{blo(yt430.y), bhi(yt430.y)}), u433a); const unsigned x434 = pk(u433[0], u433[1]); SB();
  int so454; SADDS(so454, xb00, xo2);
  x00_8 = ld16(ws, vo0 + 0, so454, SC1);
  WAITC(16, 2);
  const f32x2 u436a = {blo(qx1_1.x), bhi(qx1_1.x)}, u436 = PFMA(PFMA(u436a, (f32x2{__uint_as_float(qs1_1.x), __uint_as_float(qs1_1.x)}), (f32x2{__uint_as_float(qs1_1.y), __uint_as_float(qs1_1.y)})), (f32x2{blo(yt435.x), bhi(yt435.x)}), u436a); const unsigned x437 = pk(u436[0], u436[1]); SB();
  const f32x2 u438a = {blo(qx1_1.y), bhi(qx1_1.y)}, u438 = PFMA(PFMA(u438a, (f32x2{__uint_as_float(qs1_1.x), __uint_as_float(qs1_1.x)}), (f32x2{__uint_as_float(qs1_1.y), __uint_as_float(qs1_1.y)})), (f32x2{blo(yt435.y), bhi(yt435.y)}), u438a); const unsigned x439 = pk(u438[0], u438[1]); SB();
  x00_9 = ld16(ws, vo0 + 1024, so454, SC1);
  WAITC(16, 1);
  const f32x2 u441a = {blo(qx1_2.x), bhi(qx1_2.x)}, u441 = PFMA(PFMA(u441a, (f32x2{__uint_as_float(qs1_2.x), __uint_as_float(qs1_2.x)}), (f32x2{__uint_as_float(qs1_2.y), __uint_as_float(qs1_2.y)})), (f32x2{blo(yt440.x), bhi(yt440.x)}), u441a); const unsigned x442 = pk(u441[0], u441[1]); SB();
  const f32x2 u443a = {blo(qx1_2.y), bhi(qx1_2.y)}, u443 = PFMA(PFMA(u443a, (f32x2{__uint_as_float(qs1_2.x), __uint_as_float(qs1_2.x)}), (f32x2{__uint_as_float(qs1_2.y), __uint_as_float(qs1_2.y)})), (f32x2{blo(yt440.y), bhi(yt440.y)}), u443a); const unsigned x444 = pk(u443[0], u443[1]); SB();
  x00_10 = ld16(ws, vo0 + 2048, so454, SC1);
  WAITC(16, 0);
  const f32x2 u446a = {blo(qx1_3.x), bhi(qx1_3.x)}, u446 = PFMA(PFMA(u446a, (f32x2{__uint_as_float(qs1_3.x), __uint_as_float(qs1_3.x)}), (f32x2{__uint_as_float(qs1_3.y), __uint_as_float(qs1_3.y)})), (f32x2{blo(yt445.x), bhi(yt445.x)}), u446a); const unsigned x447 = pk(u446[0], u446[1]); SB();
  const f32x2 u448a = {blo(qx1_3.y), bhi(qx1_3.y)}, u448 = PFMA(PFMA(u448a, (f32x2{__uint_as_float(qs1_3.x), __uint_as_float(qs1_3.x)}), (f32x2{__uint_as_float(qs1_3.y), __uint_as_float(qs1_3.y)})), (f32x2{blo(yt445.y), bhi(yt445.y)}), u448a); const unsigned x449 = pk(u448[0], u448[1]); SB();
  x00_11 = ld16(ws, vo0 + 3072, so454, SC1);
  int so455; SADDS(so455, xb00, xo3);
  x00_12 = ld16(ws, vo0 + 0, so455, SC1);
  int so450; SADD(so450, x1b, 1310720);
  st16(v4u{x432, x434, x437, x439}, ws, vqz, so450);
  int so451; SADD(so451, yb, 1310720);
  st16(cat(yt430, yt435), ws, vqz, so451);
  int so452; SADD(so452, x1b, 1474560);
  st16(v4u{x442, x444, x447, x449}, ws, vqz, so452);
  int so453; SADD(so453, yb, 1474560);
  st16(cat(yt440, yt445), ws, vqz, so453);
  } else {
  #define WUNIT_OUT(u_) do { const int wj = 8 * (ne - 28) + (u_), wkk = (wj * 6554) >> 16, wcq = wj - 10 * wkk; \
    const int ra = 4 * (lane >> 4) + ((lane >> 2) & 3), fa = ra & 6; \
    const lptr a0 = L + L_WST + (u_) * 4096 + ra * 128 + (lane & 1) * 8; \
    v4u f[4]; \
    _Pragma("unroll") for (int t = 0; t < 4; ++t) { const int pc_ = ((2 * t + ((lane >> 1) & 1)) ^ fa) << 4; f[t] = cat(trd(a0 + pc_), trd(a0 + 2048 + pc_)); } \
    const int ob = wj < 540 ? O_W2T + wkk * 5120 : OOB; \
    _Pragma("unroll") for (int t = 0; t < 4; ++t) { const int ct = 4 * wcq + t; st16(f[t], ws, vo0, ob + ct * 1024 + ((ct * 52) >> 8) * 265 * 1024); } } while (0)
  asm volatile("s_waitcnt vmcnt(0)\n\ts_barrier" ::: "memory");
  ARRIVE(va8, C_S0W * 256);
  if (ne >= 28) {
    const int rr_ = lane >> 3, v0 = rr_ * D * 2 + (((lane & 7) ^ (rr_ & 6)) << 4);
    const v4u wsrd = SRD(pw2, D * F * 2);
#pragma unroll 1
    for (int u = w; u < 8; u += 4) {
      const int wj = 8 * (ne - 28) + u, wkk = (wj * 6554) >> 16, so = 32 * wkk * D * 2 + (wj - 10 * wkk) * 128, ld = L_WST + u * 4096;
      DMAI(v0, wsrd, so, ld, 0); DMAI(v0, wsrd, so + 8 * D * 2, ld + 1024, 0); DMAI(v0, wsrd, so + 16 * D * 2, ld + 2048, 0); DMAI(v0, wsrd, so + 24 * D * 2, ld + 3072, 0);
    }
    asm volatile("s_waitcnt vmcnt(0)" ::: "memory");
#pragma unroll 1
    for (int u = w; u < 8; u += 4) WUNIT_OUT(u);
  }
  asm volatile("s_waitcnt vmcnt(0)\n\ts_barrier" ::: "memory");
  ARRIVE(va8, C_S0V * 256);

    if (r < 24) {
    const int e = ne;
    if (e < 27) {
      const v4u w1srd = SRD(pw1, D * F * 2);
      const int v0 = (lane >> 3) * F * 2 + (lane & 7) * 16;
#pragma unroll 1
      for (int i = 0; i < 20; ++i) DMAI(v0, w1srd, (w * 160 + i * 8) * F * 2 + e * 128, (w * 20 + i) * 1024, 0);
      const v2u bv = ld8(mk(pb1, F * 2), (lane >> 4) * 8, (4 * e + w) * 32, 0);
      asm volatile("s_waitcnt vmcnt(0)\n\ts_barrier" ::: "memory");
      const lptr a = L + (8 * (lane >> 4) + ((lane >> 2) & 3)) * 128 + w * 32 + (lane & 3) * 8;
      f32x4 cs = {0.f, 0.f, 0.f, 0.f};
      asm volatile("s_nop 1" : "+v"(cs));
#pragma unroll 1
      for (int k = 0; k < 20; k += 5) {
        v4u f[5];
#pragma unroll
        for (int i = 0; i < 5; ++i) f[i] = cat(trd(a + (k + i) * 4096), trd(a + (k + i) * 4096 + 512));
#pragma unroll
        for (int i = 0; i < 5; ++i) MFV(cs, f[i], ones);
      }
      NOP8(); TIE(cs);
      const f32x4 o_ = (lane & 1) ? unp(bv) * 1.4426950408889634f : cs;
      st16(__builtin_bit_cast(v4u, o_), ws, (lane & 14) == 0 ? (lane & 1) * 64 + (lane >> 4) * 16 : OOB, O_CS + (4 * e + w) * 128);
    }
    asm volatile("s_waitcnt vmcnt(0)\n\ts_barrier" ::: "memory");
    ARRIVE(va8, C_S1A * 256);
  }

  if (w == 0) spin<4>(ws, vp8, C_S0W * 256, 32, 0xffull); BARRIER();
  int so456; SMUL(so456, xo0, 7);
  int so457; SADDS(so457, w1s, so456);
  SETM0(w1d, 0);
  asm volatile("s_nop 0");
  DMA(so457, 0);
  DMA(so457, 1024);
  DMA(so457, 2048);
  DMA(so457, 3072);
  int so458; SADD(so458, so457, 4096);
  SETM0(w1d, 4096);
  asm volatile("s_nop 0");
  DMA(so458, 0);
  DMA(so458, 1024);
  DMA(so458, 2048);
  int so461; SMUL(so461, xo1, 7);
  int so462; SADDS(so462, w1s, so461);
  SETM0(w1d, 28672);
  asm volatile("s_nop 0");
  DMA(so462, 0);
  DMA(so462, 1024);
  DMA(so462, 2048);
  DMA(so462, 3072);
  int so463; SADD(so463, so462, 4096);
  SETM0(w1d, 32768);
  asm volatile("s_nop 0");
  DMA(so463, 0);
  DMA(so463, 1024);
  DMA(so463, 2048);
  int so466; SMUL(so466, xo2, 7);
  int so467; SADDS(so467, w1s, so466);
  SETM0(w1d, 57344);
  asm volatile("s_nop 0");
  DMA(so467, 0);
  DMA(so467, 1024);
  DMA(so467, 2048);
  DMA(so467, 3072);
  int so468; SADD(so468, so467, 4096);
  SETM0(w1d, 61440);
  asm volatile("s_nop 0");
  DMA(so468, 0);
  DMA(so468, 1024);
  DMA(so468, 2048);
  int so471; SMUL(so471, xo3, 7);
  int so472; SADDS(so472, w1s, so471);
  SETM0(w1d, 86016);
  asm volatile("s_nop 0");
  DMA(so472, 0);
  DMA(so472, 1024);
  DMA(so472, 2048);
  DMA(so472, 3072);
  int so473; SADD(so473, so472, 4096);
  SETM0(w1d, 90112);
  asm volatile("s_nop 0");
  DMA(so473, 0);
  DMA(so473, 1024);
  DMA(so473, 2048);
  int so476; SMUL(so476, xo4, 7);
  int so477; SADDS(so477, w1s, so476);
  SETM0(w1d, 114688);
  asm volatile("s_nop 0");
  DMA(so477, 0);
  DMA(so477, 1024);
  DMA(so477, 2048);
  DMA(so477, 3072);
  int so478; SADD(so478, so477, 4096);
  SETM0(w1d, 118784);
  asm volatile("s_nop 0");
  DMA(so478, 0);
  DMA(so478, 1024);
  DMA(so478, 2048);
  if (w == 0) spin<4>(ws, vp8, C_S0V * 256, 12, 0xffull); BARRIER();
  int so481; SMUL(so481, ko0, 5);
  int so482; SADDS(so482, w2p, so481);
  W0 = ld16(ws, vo0 + 0, so482, SC1);
  W1 = ld16(ws, vo0 + 1024, so482, SC1);
  int so483; SADDS(so483, w2q, so481);
  W2 = ld16(ws, vo0 + 0, so483, SC1);
  W3 = ld16(ws, vo0 + 1024, so483, SC1);
  WAITC(0, 0);
  if (w == 0) spin<8>(ws, vp8, C_S1A * 256, 24, 0xffull); BARRIER();
  cbv = CB_LOAD();
  int so484; SADDS(so484, xb00, xo0);
  x00_0 = ld16(ws, vo0 + 0, so484, SC1);
  x00_1 = ld16(ws, vo0 + 1024, so484, SC1);
  x00_2 = ld16(ws, vo0 + 2048, so484, SC1);
  x00_3 = ld16(ws, vo0 + 3072, so484, SC1);
  int so485; SADDS(so485, xb00, xo1);
  x00_4 = ld16(ws, vo0 + 0, so485, SC1);
  x00_5 = ld16(ws, vo0 + 1024, so485, SC1);
  x00_6 = ld16(ws, vo0 + 2048, so485, SC1);
  x00_7 = ld16(ws, vo0 + 3072, so485, SC1);
  int so486; SADDS(so486, xb00, xo2);
  x00_8 = ld16(ws, vo0 + 0, so486, SC1);
  x00_9 = ld16(ws, vo0 + 1024, so486, SC1);
  x00_10 = ld16(ws, vo0 + 2048, so486, SC1);
  x00_11 = ld16(ws, vo0 + 3072, so486, SC1);
  int so487; SADDS(so487, xb00, xo3);
  x00_12 = ld16(ws, vo0 + 0, so487, SC1);
  WAITC(0, 0);
  }
    int va8c = isconv ? va8 : OOB; TIE(va8c);
  const int gl = (grp >> 1) * 57 + C_G + (grp & 1) * 6, gl2 = xc * 57 + C_G + (grp2 & 1) * 6;
  lptr wb0 = L + lane * 16, wb1 = wb0 + 64512, wb2 = wb0 + 129024;
  lptr cbp = L + L_RED + (lane >> 4) * 16;
  TIE(wb0); TIE(wb1); TIE(wb2); TIE(cbp);
  const bool isdiag = (lane >> 4) == ((lane & 15) >> 2);
  const f32x2 kone = {1.f, 1.f}, kln2 = {0.6931471805599453f, 0.6931471805599453f}, km2 = {-1.3862943611198906f, -1.3862943611198906f};
  const int k0 = (ft0 + odd) >> 1, t6 = mem >= 12 ? OOB : (odd ? ((ft0 - 1) >> 1) * 1024 + 512 : ((ft0 + 6) >> 1) * 1024);
  const int hs00 = O_H1 + (mtg * 54 + k0) * 1024, hs01 = hs00 + 64 * 54 * 1024, hs10 = hs00 + (O_H2 - O_H1), hs11 = hs01 + (O_H2 - O_H1);
  const int ht00 = O_H1 + mtg * 54 * 1024 + t6, ht01 = ht00 + 64 * 54 * 1024, ht10 = ht00 + (O_H2 - O_H1), ht11 = ht01 + (O_H2 - O_H1);
  const int xb10 = O_X3 + mtg * 20480, xb11 = xb10 + 64 * 20480;
  const int spb0 = O_ST3 + mtg * 2048, spb1 = spb0 + 64 * 2048;
  const int mto = 4 * grp2 + 2 * ri + (w & 1), ct0 = 5 * ci + 3 * hiw, part = 2 * ci + hiw;
  const int hb001 = O_H1 + (mto * 54 + ks) * 1024, hb000 = O_H1 + ((mto ^ 1) * 54 + ks) * 1024;
  const int hb010 = hb000 + 64 * 54 * 1024, hb011 = hb001 + 64 * 54 * 1024;
  const int hb100 = hb000 + (O_H2 - O_H1), hb101 = hb001 + (O_H2 - O_H1), hb110 = hb010 + (O_H2 - O_H1), hb111 = hb011 + (O_H2 - O_H1);
  const int ex0 = O_X1 + mto * 20480 + ct0 * 512, ex1 = ex0 + 64 * 20480, ey0 = ex0 + SD2, ey1 = ex1 + SD2, e3o = hiw ? OOB : 1024;
#define ex30 (ex0 + e3o)
#define ex31 (ex1 + e3o)
#define ey30 (ey0 + e3o)
#define ey31 (ey1 + e3o)
  const int gb0 = O_ST2 + mto * 4096, gb1 = gb0 + 64 * 4096;
  const int vxe = (lane >> 4) * 128 + (lane & 7) * 16 + ((lane >> 3) & 1) * 8;
  int vl8 = lane * 8, vha = w == 3 ? lane * 16 : lane * 8, vhb = vha + (w == 3 ? 8 : 512); TIE(vl8); TIE(vha); TIE(vhb);
  u64 mlo = 0x00ff00ff00ff00ffull, mhi = ~mlo; TIES(mlo); TIES(mhi);
  const int vx4 = ((lane >> 4) & 1) * 512 + (lane >> 5) * 256 + (lane & 15) * 16, vx4e = (lane & 16) ? OOB : vx4;
  const int vg = (lane >> 4) * 1024 + (lane & 15) * 16, vsp = (lane >> 4) * 512 + (lane & 15) * 8;
  const lptr redl = L + L_RED + lane * 16;
  const lptr xw0 = redl + w * 5120 + hiw * 3072, xw2 = redl + w * 5120 - hiw * 2048, xr0 = redl + (w ^ 1) * 5120 + hiw * 3072, xr1 = redl + (w ^ 3) * 5120 + hiw * 3072;
  const lptr yr2a = redl + (w ^ 3) * 5120 + (1 - hiw) * 3072, yr2b = redl + (w ^ 1) * 5120 + (1 - hiw) * 3072;
  const lptr yq0 = hiw ? yr2a : yr2b, yq1 = hiw ? yr2a + 1024 : yr2a;
  const rsrc_t ro = mk(pout, SD2);
#define EPI2A(hf) do { r0##hf##_0 += unp(e1##hf##_0); r0##hf##_1 += unp(e1##hf##_1); r0##hf##_2 += unp(e1##hf##_2); \
    if (hiw) r0##hf##_2 = f32x4{0.f, 0.f, 0.f, 0.f}; \
    const f32x4 a_ = (r0##hf##_0 + r0##hf##_1) + r0##hf##_2; \
    const f32x4 q_ = __builtin_elementwise_fma(r0##hf##_2, r0##hf##_2, __builtin_elementwise_fma(r0##hf##_1, r0##hf##_1, r0##hf##_0 * r0##hf##_0)); \
    const float s1_ = xsum_g((a_[0] + a_[1]) + (a_[2] + a_[3])), s2_ = xsum_g((q_[0] + q_[1]) + (q_[2] + q_[3])); \
    st16(v4u{__float_as_uint(s1_), 1u, __float_as_uint(s2_), 1u}, ws, lane < 16 ? vo0 : OOB, gb##hf + part * 256); } while (0)
#define GTAGS(hf) ((g##hf##_0.y & g##hf##_0.w & g##hf##_1.y & g##hf##_1.w & g##hf##_2.y & g##hf##_2.w & g##hf##_3.y & g##hf##_3.w) != 1u)
#define GATHER_RETRY(hf) do { if (__builtin_amdgcn_ballot_w64(GTAGS(hf)) != 0) { \
      g##hf##_0 = ld16(ws, vg, gb##hf, SC1); g##hf##_1 = ld16(ws, vg + 256, gb##hf, SC1); g##hf##_2 = ld16(ws, vg + 512, gb##hf, SC1); g##hf##_3 = ld16(ws, vg + 768, gb##hf, SC1); } } while (0)
#define GATHER_CK(hf) do { if (__builtin_expect(__builtin_amdgcn_ballot_w64(GTAGS(hf)) != 0, 0)) { \
    _Pragma("nounroll") for (unsigned spins = 0;; ++spins) { asm volatile("; SPIN" ::: "memory"); \
      g##hf##_0 = ld16(ws, vg, gb##hf, SC1); g##hf##_1 = ld16(ws, vg + 256, gb##hf, SC1); g##hf##_2 = ld16(ws, vg + 512, gb##hf, SC1); g##hf##_3 = ld16(ws, vg + 768, gb##hf, SC1); \
      if (__builtin_amdgcn_ballot_w64(GTAGS(hf)) == 0) break; \
      if (spins > SPIN_CAP) { __builtin_amdgcn_raw_buffer_store_b32(999, ws, O_TMO, 0, SC1); break; } } } } while (0)
#define X3T(hf, t) const f32x4 v##t = __builtin_elementwise_fma(__builtin_elementwise_fma(r0##hf##_##t, r4_, n4_), unp(ey##hf##_##t), r0##hf##_##t); \
    x3##hf##_##t = v2u{pk(v##t[0], v##t[1]), pk(v##t[2], v##t[3])}
#define EPI2B(hf) v2u x3##hf##_0, x3##hf##_1, x3##hf##_2; do { float rs_, nm_; \
    lnstat(xsum_g((__uint_as_float(g##hf##_0.x) + __uint_as_float(g##hf##_1.x)) + (__uint_as_float(g##hf##_2.x) + __uint_as_float(g##hf##_3.x))), \
           xsum_g((__uint_as_float(g##hf##_0.z) + __uint_as_float(g##hf##_1.z)) + (__uint_as_float(g##hf##_2.z) + __uint_as_float(g##hf##_3.z))), rs_, nm_); \
    const f32x4 r4_ = {rs_, rs_, rs_, rs_}, n4_ = {nm_, nm_, nm_, nm_}; \
    X3T(hf, 0); X3T(hf, 1); X3T(hf, 2); \
    st16(rowpair(x3##hf##_0, x3##hf##_1), ws, vx4, ex##hf + (O_X3 - O_X1)); st16(rowpair(x3##hf##_2, x3##hf##_2), ws, vx4e, ex3##hf + (O_X3 - O_X1)); \
    const f32x4 a_ = (v0 + v1) + v2, q_ = __builtin_elementwise_fma(v2, v2, __builtin_elementwise_fma(v1, v1, v0 * v0)); \
    const float s1_ = xsum_g((a_[0] + a_[1]) + (a_[2] + a_[3])), s2_ = xsum_g((q_[0] + q_[1]) + (q_[2] + q_[3])); \
    st8(v2u{__float_as_uint(s1_), __float_as_uint(s2_)}, ws, lane < 16 ? vl8 : OOB, O_ST3 + ((mto + 64 * hf) * 16 + part) * 128); } while (0)
#define OUTT(hf, t) const f32x4 v##t = r1##hf##_##t + unp(x3##hf##_##t); const v2u o##t = {pk(v##t[0], v##t[1]), pk(v##t[2], v##t[3])}
#define EPI_OUT(hf, aux) do { OUTT(hf, 0); OUTT(hf, 1); OUTT(hf, 2); \
    const int vo_ = (lane & 15) * (D * 2) + ((lane >> 4) & 1) * 32 + (lane >> 5) * 16, so_ = (mto + 64 * hf) * 16 * D * 2 + ct0 * 32; \
    v4u a_ = rowpair(o0, o1), b_ = rowpair(o2, o2); TIE(a_); TIE(b_); \
    __builtin_amdgcn_raw_buffer_store_b128(a_, ro, vo_, so_, aux); \
    __builtin_amdgcn_raw_buffer_store_b128(b_, ro, (lane & 16) ? OOB : vo_, hiw ? OOB : so_ + 64, aux); \
    asm volatile("s_nop 1" :: "v"(a_), "v"(b_)); } while (0)
#define ST2_CLEAR() st16(v4u{0u, 0u, 0u, 0u}, ws, lane < 32 ? (lane >> 4) * 64 * 4096 + (lane & 15) * 16 : OOB, gb0 + part * 256)
  const int vtx = lane < 20 ? (((r & 7) * 4 + w) * 20 + lane) * 128 : OOB;
#define TOUCH_X(v, L_, hf) const unsigned v = __builtin_amdgcn_raw_buffer_load_b32(ws, vtx, (L_ ? O_X3 : O_X1) + (64 * hf + 4 * grp) * 20480, SC1)
  const int vth = lane < 27 ? ((ci * 4 + w) * 27 + lane) * 128 : OOB;
#define TOUCH_H(v, L_, hf) const unsigned v = __builtin_amdgcn_raw_buffer_load_b32(ws, vth, (L_ ? O_H2 : O_H1) + (64 * hf + 4 * grp2 + 2 * ri) * 55296, SC1)

  const v4u w532 = LDS16(wb0 + 0);
  const v4u w533 = LDS16(wb0 + 1024);
  const v4u w534 = LDS16(wb0 + 2048);
  const v4u w535 = LDS16(wb0 + 3072);
  const v4u w536 = LDS16(wb0 + 4096);
  const v4u w537 = LDS16(wb0 + 5120);
  const v4u w538 = LDS16(wb0 + 6144);
  STAMP(8);
  WAITC(16, 15);
  unsigned f0_0a0, f0_0a1; FIXA(f0_0, x00_0, mlo);
  unsigned f0_0b0, f0_0b1; FIXB(f0_0, x00_0, mhi);
  WAITC(15, 15);
  unsigned f0_1a0, f0_1a1; FIXA(f0_1, x00_1, mlo);
  unsigned f0_1b0, f0_1b1; FIXB(f0_1, x00_1, mhi);
  WAITC(63, 6);
  f32x4 a00_0; MFV0(a00_0, w532, (v4u{f0_0a0, f0_0a1, f0_0b0, f0_0b1}));
  int so496; SMUL(so496, ko0, 5);
  const v4u w539 = LDS16(wb0 + 7168);
  int so497; SADDS(so497, w2q, so496);
  WAITC(63, 6);
  f32x4 a00_1; MFV0(a00_1, w533, (v4u{f0_0a0, f0_0a1, f0_0b0, f0_0b1}));
  WAITC(63, 4);
  const v4u w540 = LDS16(wb0 + 8192);
  W4 = ld16(ws, vo0 + 2048, so497, SC1);
  f32x4 a00_2; MFV0(a00_2, w534, (v4u{f0_0a0, f0_0a1, f0_0b0, f0_0b1}));
  int so498; SMUL(so498, ko1, 5);
  const v4u w541 = LDS16(wb0 + 9216);
  int so499; SADDS(so499, w2p, so498);
  f32x4 a00_3; MFV0(a00_3, w535, (v4u{f0_0a0, f0_0a1, f0_0b0, f0_0b1}));
  WAITC(63, 5);
  const v4u w542 = LDS16(wb0 + 10240);
  f32x4 a00_4; MFV0(a00_4, w536, (v4u{f0_0a0, f0_0a1, f0_0b0, f0_0b1}));
  WAITC(63, 5);
  const v4u w543 = LDS16(wb0 + 11264);
  W5 = ld16(ws, vo0 + 0, so499, SC1);
  f32x4 a00_5; MFV0(a00_5, w537, (v4u{f0_0a0, f0_0a1, f0_0b0, f0_0b1}));
  WAITC(63, 5);
  const v4u w544 = LDS16(wb0 + 12288);
  f32x4 a00_6; MFV0(a00_6, w538, (v4u{f0_0a0, f0_0a1, f0_0b0, f0_0b1}));
  WAITC(63, 5);
  const v4u w545 = LDS16(wb0 + 13312);
  f32x4 sa0; MFV0(sa0, ones, (v4u{f0_0a0, f0_0a1, f0_0b0, f0_0b1}));
  WAITC(16, 15);
  unsigned f0_2a0, f0_2a1; FIXA(f0_2, x00_2, mlo);
  f32x4 sq0; MFV0(sq0, (v4u{f0_0a0, f0_0a1, f0_0b0, f0_0b1}), (v4u{f0_0a0, f0_0a1, f0_0b0, f0_0b1}));
  unsigned f0_2b0, f0_2b1; FIXB(f0_2, x00_2, mhi);
  MFV(a00_0, w539, (v4u{f0_1a0, f0_1a1, f0_1b0, f0_1b1}));
  WAITC(63, 4);
  const v4u w546 = LDS16(wb0 + 14336);
  W6 = ld16(ws, vo0 + 1024, so499, SC1);
  MFV(a00_1, w540, (v4u{f0_1a0, f0_1a1, f0_1b0, f0_1b1}));
  int so500; SADDS(so500, w2q, so498);
  const v4u w547 = LDS16(wb0 + 15360);
  MFV(a00_2, w541, (v4u{f0_1a0, f0_1a1, f0_1b0, f0_1b1}));
  WAITC(63, 5);
  const v4u w548 = LDS16(wb0 + 16384);
  MFV(a00_3, w542, (v4u{f0_1a0, f0_1a1, f0_1b0, f0_1b1}));
  WAITC(63, 5);
  const v4u w549 = LDS16(wb0 + 17408);
  W7 = ld16(ws, vo0 + 0, so500, SC1);
  MFV(a00_4, w543, (v4u{f0_1a0, f0_1a1, f0_1b0, f0_1b1}));
  WAITC(63, 5);
  const v4u w550 = LDS16(wb0 + 18432);
  MFV(a00_5, w544, (v4u{f0_1a0, f0_1a1, f0_1b0, f0_1b1}));
  WAITC(63, 5);
  const v4u w551 = LDS16(wb0 + 19456);
  MFV(a00_6, w545, (v4u{f0_1a0, f0_1a1, f0_1b0, f0_1b1}));
  WAITC(63, 5);
  const v4u w552 = LDS16(wb0 + 20480);
  W8 = ld16(ws, vo0 + 1024, so500, SC1);
  MFV(sa0, ones, (v4u{f0_1a0, f0_1a1, f0_1b0, f0_1b1}));
  WAITC(18, 15);
  unsigned f0_3a0, f0_3a1; FIXA(f0_3, x00_3, mlo);
  MFV(sq0, (v4u{f0_1a0, f0_1a1, f0_1b0, f0_1b1}), (v4u{f0_1a0, f0_1a1, f0_1b0, f0_1b1}));
  unsigned f0_3b0, f0_3b1; FIXB(f0_3, x00_3, mhi);
  MFV(a00_0, w546, (v4u{f0_2a0, f0_2a1, f0_2b0, f0_2b1}));
  WAITC(63, 4);
  const v4u w553 = LDS16(wb0 + 21504);
  W9 = ld16(ws, vo0 + 2048, so500, SC1);
  MFV(a00_1, w547, (v4u{f0_2a0, f0_2a1, f0_2b0, f0_2b1}));
  int so501; SMUL(so501, ko2, 5);
  const v4u w554 = LDS16(wb0 + 22528);
  int so502; SADDS(so502, w2p, so501);
  MFV(a00_2, w548, (v4u{f0_2a0, f0_2a1, f0_2b0, f0_2b1}));
  WAITC(63, 5);
  const v4u w555 = LDS16(wb0 + 23552);
  MFV(a00_3, w549, (v4u{f0_2a0, f0_2a1, f0_2b0, f0_2b1}));
  WAITC(63, 5);
  const v4u w556 = LDS16(wb0 + 24576);
  W10 = ld16(ws, vo0 + 0, so502, SC1);
  MFV(a00_4, w550, (v4u{f0_2a0, f0_2a1, f0_2b0, f0_2b1}));
  WAITC(63, 5);
  const v4u w557 = LDS16(wb0 + 25600);
  MFV(a00_5, w551, (v4u{f0_2a0, f0_2a1, f0_2b0, f0_2b1}));
  WAITC(63, 5);
  const v4u w558 = LDS16(wb0 + 26624);
  MFV(a00_6, w552, (v4u{f0_2a0, f0_2a1, f0_2b0, f0_2b1}));
  WAITC(63, 5);
  const v4u w559 = LDS16(wb0 + 27648);
  W11 = ld16(ws, vo0 + 1024, so502, SC1);
  MFV(sa0, ones, (v4u{f0_2a0, f0_2a1, f0_2b0, f0_2b1}));
  WAITC(20, 15);
  unsigned f0_4a0, f0_4a1; FIXA(f0_4, x00_4, mlo);
  MFV(sq0, (v4u{f0_2a0, f0_2a1, f0_2b0, f0_2b1}), (v4u{f0_2a0, f0_2a1, f0_2b0, f0_2b1}));
  unsigned f0_4b0, f0_4b1; FIXB(f0_4, x00_4, mhi);
  MFV(a00_0, w553, (v4u{f0_3a0, f0_3a1, f0_3b0, f0_3b1}));
  int so503; SADDS(so503, w2q, so501);
  const v4u w560 = LDS16(wb0 + 28672);
  W12 = ld16(ws, vo0 + 0, so503, SC1);
  WAITC(63, 6);
  MFV(a00_1, w554, (v4u{f0_3a0, f0_3a1, f0_3b0, f0_3b1}));
  WAITC(63, 5);
  const v4u w561 = LDS16(wb0 + 29696);
  MFV(a00_2, w555, (v4u{f0_3a0, f0_3a1, f0_3b0, f0_3b1}));
  WAITC(63, 5);
  const v4u w562 = LDS16(wb0 + 30720);
  MFV(a00_3, w556, (v4u{f0_3a0, f0_3a1, f0_3b0, f0_3b1}));
  WAITC(63, 5);
  const v4u w563 = LDS16(wb0 + 31744);
  W13 = ld16(ws, vo0 + 1024, so503, SC1);
  MFV(a00_4, w557, (v4u{f0_3a0, f0_3a1, f0_3b0, f0_3b1}));
  WAITC(63, 5);
  const v4u w564 = LDS16(wb0 + 32768);
  MFV(a00_5, w558, (v4u{f0_3a0, f0_3a1, f0_3b0, f0_3b1}));
  WAITC(63, 5);
  const v4u w565 = LDS16(wb0 + 33792);
  MFV(a00_6, w559, (v4u{f0_3a0, f0_3a1, f0_3b0, f0_3b1}));
  WAITC(63, 5);
  const v4u w566 = LDS16(wb0 + 34816);
  W14 = ld16(ws, vo0 + 2048, so503, SC1);
  MFV(sa0, ones, (v4u{f0_3a0, f0_3a1, f0_3b0, f0_3b1}));
  WAITC(22, 15);
  unsigned f0_5a0, f0_5a1; FIXA(f0_5, x00_5, mlo);
  MFV(sq0, (v4u{f0_3a0, f0_3a1, f0_3b0, f0_3b1}), (v4u{f0_3a0, f0_3a1, f0_3b0, f0_3b1}));
  unsigned f0_5b0, f0_5b1; FIXB(f0_5, x00_5, mhi);
  MFV(a00_0, w560, (v4u{f0_4a0, f0_4a1, f0_4b0, f0_4b1}));
  int so504; SMUL(so504, ko3, 5);
  const v4u w567 = LDS16(wb0 + 35840);
  int so505; SADDS(so505, w2p, so504);
  WAITC(63, 6);
  MFV(a00_1, w561, (v4u{f0_4a0, f0_4a1, f0_4b0, f0_4b1}));
  WAITC(63, 5);
  const v4u w568 = LDS16(wb0 + 36864);
  W15 = ld16(ws, vo0 + 0, so505, SC1);
  MFV(a00_2, w562, (v4u{f0_4a0, f0_4a1, f0_4b0, f0_4b1}));
  WAITC(63, 5);
  const v4u w569 = LDS16(wb0 + 37888);
  MFV(a00_3, w563, (v4u{f0_4a0, f0_4a1, f0_4b0, f0_4b1}));
  WAITC(63, 5);
  const v4u w570 = LDS16(wb0 + 38912);
  MFV(a00_4, w564, (v4u{f0_4a0, f0_4a1, f0_4b0, f0_4b1}));
  WAITC(63, 4);
  const v4u w571 = LDS16(wb0 + 39936);
  W16 = ld16(ws, vo0 + 1024, so505, SC1);
  MFV(a00_5, w565, (v4u{f0_4a0, f0_4a1, f0_4b0, f0_4b1}));
  int so506; SADDS(so506, w2q, so504);
  const v4u w572 = LDS16(wb0 + 40960);
  MFV(a00_6, w566, (v4u{f0_4a0, f0_4a1, f0_4b0, f0_4b1}));
  WAITC(63, 5);
  const v4u w573 = LDS16(wb0 + 41984);
  MFV(sa0, ones, (v4u{f0_4a0, f0_4a1, f0_4b0, f0_4b1}));
  WAITC(23, 15);
  unsigned f0_6a0, f0_6a1; FIXA(f0_6, x00_6, mlo);
  MFV(sq0, (v4u{f0_4a0, f0_4a1, f0_4b0, f0_4b1}), (v4u{f0_4a0, f0_4a1, f0_4b0, f0_4b1}));
  unsigned f0_6b0, f0_6b1; FIXB(f0_6, x00_6, mhi);
  MFV(a00_0, w567, (v4u{f0_5a0, f0_5a1, f0_5b0, f0_5b1}));
  WAITC(63, 5);
  const v4u w574 = LDS16(wb0 + 43008);
  W17 = ld16(ws, vo0 + 0, so506, SC1);
  MFV(a00_1, w568, (v4u{f0_5a0, f0_5a1, f0_5b0, f0_5b1}));
  WAITC(63, 5);
  const v4u w575 = LDS16(wb0 + 44032);
  MFV(a00_2, w569, (v4u{f0_5a0, f0_5a1, f0_5b0, f0_5b1}));
  WAITC(63, 5);
  const v4u w576 = LDS16(wb0 + 45056);
  MFV(a00_3, w570, (v4u{f0_5a0, f0_5a1, f0_5b0, f0_5b1}));
  WAITC(63, 5);
  const v4u w577 = LDS16(wb0 + 46080);
  W18 = ld16(ws, vo0 + 1024, so506, SC1);
  MFV(a00_4, w571, (v4u{f0_5a0, f0_5a1, f0_5b0, f0_5b1}));
  WAITC(63, 5);
  const v4u w578 = LDS16(wb0 + 47104);
  CB_STORE();
  MFV(a00_5, w572, (v4u{f0_5a0, f0_5a1, f0_5b0, f0_5b1}));
  WAITC(63, 6);
  const v4u w579 = LDS16(wb0 + 48128);
  WAITC(63, 7);
  DRAINBAR(15);
  MFV(a00_6, w573, (v4u{f0_5a0, f0_5a1, f0_5b0, f0_5b1}));
  WAITC(63, 6);
  const v4u w580 = LDS16(wb0 + 49152);
  ARRIVE(va8c, (C_S1B) * 256);
  MFV(sa0, ones, (v4u{f0_5a0, f0_5a1, f0_5b0, f0_5b1}));
  unsigned f0_7a0, f0_7a1; FIXA(f0_7, x00_7, mlo);
  MFV(sq0, (v4u{f0_5a0, f0_5a1, f0_5b0, f0_5b1}), (v4u{f0_5a0, f0_5a1, f0_5b0, f0_5b1}));
  unsigned f0_7b0, f0_7b1; FIXB(f0_7, x00_7, mhi);
  MFV(a00_0, w574, (v4u{f0_6a0, f0_6a1, f0_6b0, f0_6b1}));
  int so494; SADDS(so494, xb00, xo3);
  const v4u w581 = LDS16(wb0 + 50176);
  v4u x00_13 = ld16(ws, vo0 + 1024, so494, SC1);
  WAITC(63, 7);
  MFV(a00_1, w575, (v4u{f0_6a0, f0_6a1, f0_6b0, f0_6b1}));
  WAITC(63, 6);
  const v4u w582 = LDS16(wb0 + 51200);
  MFV(a00_2, w576, (v4u{f0_6a0, f0_6a1, f0_6b0, f0_6b1}));
  WAITC(63, 6);
  const v4u w583 = LDS16(wb0 + 52224);
  MFV(a00_3, w577, (v4u{f0_6a0, f0_6a1, f0_6b0, f0_6b1}));
  WAITC(63, 6);
  const v4u w584 = LDS16(wb0 + 53248);
  v4u x00_14 = ld16(ws, vo0 + 2048, so494, SC1);
  MFV(a00_4, w578, (v4u{f0_6a0, f0_6a1, f0_6b0, f0_6b1}));
  WAITC(63, 5);
  const v4u w585 = LDS16(wb0 + 54272);
  MFV(a00_5, w579, (v4u{f0_6a0, f0_6a1, f0_6b0, f0_6b1}));
  WAITC(63, 5);
  const v4u w586 = LDS16(wb0 + 55296);
  MFV(a00_6, w580, (v4u{f0_6a0, f0_6a1, f0_6b0, f0_6b1}));
  WAITC(63, 5);
  const v4u w587 = LDS16(wb0 + 56320);
  v4u x00_15 = ld16(ws, vo0 + 3072, so494, SC1);
  MFV(sa0, ones, (v4u{f0_6a0, f0_6a1, f0_6b0, f0_6b1}));
  unsigned f0_8a0, f0_8a1; FIXA(f0_8, x00_8, mlo);
  MFV(sq0, (v4u{f0_6a0, f0_6a1, f0_6b0, f0_6b1}), (v4u{f0_6a0, f0_6a1, f0_6b0, f0_6b1}));
  unsigned f0_8b0, f0_8b1; FIXB(f0_8, x00_8, mhi);
  MFV(a00_0, w581, (v4u{f0_7a0, f0_7a1, f0_7b0, f0_7b1}));
  int so495; SADDS(so495, xb00, xo4);
  const v4u w588 = LDS16(wb0 + 57344);
  v4u x00_16 = ld16(ws, vo0 + 0, so495, SC1);
  WAITC(63, 6);
  MFV(a00_1, w582, (v4u{f0_7a0, f0_7a1, f0_7b0, f0_7b1}));
  WAITC(63, 5);
  const v4u w589 = LDS16(wb0 + 58368);
  MFV(a00_2, w583, (v4u{f0_7a0, f0_7a1, f0_7b0, f0_7b1}));
  WAITC(63, 5);
  const v4u w590 = LDS16(wb0 + 59392);
  MFV(a00_3, w584, (v4u{f0_7a0, f0_7a1, f0_7b0, f0_7b1}));
  WAITC(63, 5);
  const v4u w591 = LDS16(wb0 + 60416);
  v4u x00_17 = ld16(ws, vo0 + 1024, so495, SC1);
  MFV(a00_4, w585, (v4u{f0_7a0, f0_7a1, f0_7b0, f0_7b1}));
  WAITC(63, 5);
  const v4u w592 = LDS16(wb0 + 61440);
  MFV(a00_5, w586, (v4u{f0_7a0, f0_7a1, f0_7b0, f0_7b1}));
  WAITC(63, 5);
  const v4u w593 = LDS16(wb0 + 62464);
  MFV(a00_6, w587, (v4u{f0_7a0, f0_7a1, f0_7b0, f0_7b1}));
  WAITC(63, 5);
  const v4u w594 = LDS16(wb0 + 63488);
  v4u x00_18 = ld16(ws, vo0 + 2048, so495, SC1);
  MFV(sa0, ones, (v4u{f0_7a0, f0_7a1, f0_7b0, f0_7b1}));
  unsigned f0_9a0, f0_9a1; FIXA(f0_9, x00_9, mlo);
  MFV(sq0, (v4u{f0_7a0, f0_7a1, f0_7b0, f0_7b1}), (v4u{f0_7a0, f0_7a1, f0_7b0, f0_7b1}));
  unsigned f0_9b0, f0_9b1; FIXB(f0_9, x00_9, mhi);
  MFV(a00_0, w588, (v4u{f0_8a0, f0_8a1, f0_8b0, f0_8b1}));
  WAITC(63, 5);
  const v4u w595 = LDS16(wb1 + 0);
  v4u x00_19 = ld16(ws, vo0 + 3072, so495, SC1);
  MFV(a00_1, w589, (v4u{f0_8a0, f0_8a1, f0_8b0, f0_8b1}));
  WAITC(63, 5);
  const v4u w596 = LDS16(wb1 + 1024);
  MFV(a00_2, w590, (v4u{f0_8a0, f0_8a1, f0_8b0, f0_8b1}));
  WAITC(63, 5);
  const v4u w597 = LDS16(wb1 + 2048);
  MFV(a00_3, w591, (v4u{f0_8a0, f0_8a1, f0_8b0, f0_8b1}));
  WAITC(63, 5);
  const v4u w598 = LDS16(wb1 + 3072);
  ST2_CLEAR();
  MFV(a00_4, w592, (v4u{f0_8a0, f0_8a1, f0_8b0, f0_8b1}));
  WAITC(63, 5);
  const v4u w599 = LDS16(wb1 + 4096);
  MFV(a00_5, w593, (v4u{f0_8a0, f0_8a1, f0_8b0, f0_8b1}));
  WAITC(63, 5);
  const v4u w600 = LDS16(wb1 + 5120);
  MFV(a00_6, w594, (v4u{f0_8a0, f0_8a1, f0_8b0, f0_8b1}));
  WAITC(63, 5);
  const v4u w601 = LDS16(wb1 + 6144);
  W19 = ld16(ws, vo0 + 2048, so506, SC1);
  MFV(sa0, ones, (v4u{f0_8a0, f0_8a1, f0_8b0, f0_8b1}));
  unsigned f0_10a0, f0_10a1; FIXA(f0_10, x00_10, mlo);
  MFV(sq0, (v4u{f0_8a0, f0_8a1, f0_8b0, f0_8b1}), (v4u{f0_8a0, f0_8a1, f0_8b0, f0_8b1}));
  unsigned f0_10b0, f0_10b1; FIXB(f0_10, x00_10, mhi);
  MFV(a00_0, w595, (v4u{f0_9a0, f0_9a1, f0_9b0, f0_9b1}));
  int so507; SMUL(so507, ko4, 5);
  const v4u w602 = LDS16(wb1 + 7168);
  int so508; SADDS(so508, w2p, so507);
  WAITC(63, 6);
  MFV(a00_1, w596, (v4u{f0_9a0, f0_9a1, f0_9b0, f0_9b1}));
  WAITC(63, 5);
  const v4u w603 = LDS16(wb1 + 8192);
  W20 = ld16(ws, vo0 + 0, so508, SC1);
  MFV(a00_2, w597, (v4u{f0_9a0, f0_9a1, f0_9b0, f0_9b1}));
  WAITC(63, 5);
  const v4u w604 = LDS16(wb1 + 9216);
  MFV(a00_3, w598, (v4u{f0_9a0, f0_9a1, f0_9b0, f0_9b1}));
  WAITC(63, 5);
  const v4u w605 = LDS16(wb1 + 10240);
  MFV(a00_4, w599, (v4u{f0_9a0, f0_9a1, f0_9b0, f0_9b1}));
  WAITC(63, 4);
  const v4u w606 = LDS16(wb1 + 11264);
  W21 = ld16(ws, vo0 + 1024, so508, SC1);
  MFV(a00_5, w600, (v4u{f0_9a0, f0_9a1, f0_9b0, f0_9b1}));
  int so509; SADDS(so509, w2q, so507);
  const v4u w607 = LDS16(wb1 + 12288);
  MFV(a00_6, w601, (v4u{f0_9a0, f0_9a1, f0_9b0, f0_9b1}));
  WAITC(63, 5);
  const v4u w608 = LDS16(wb1 + 13312);
  MFV(sa0, ones, (v4u{f0_9a0, f0_9a1, f0_9b0, f0_9b1}));
  unsigned f0_11a0, f0_11a1; FIXA(f0_11, x00_11, mlo);
  MFV(sq0, (v4u{f0_9a0, f0_9a1, f0_9b0, f0_9b1}), (v4u{f0_9a0, f0_9a1, f0_9b0, f0_9b1}));
  unsigned f0_11b0, f0_11b1; FIXB(f0_11, x00_11, mhi);
  MFV(a00_0, w602, (v4u{f0_10a0, f0_10a1, f0_10b0, f0_10b1}));
  WAITC(63, 5);
  const v4u w609 = LDS16(wb1 + 14336);
  W22 = ld16(ws, vo0 + 0, so509, SC1);
  MFV(a00_1, w603, (v4u{f0_10a0, f0_10a1, f0_10b0, f0_10b1}));
  WAITC(63, 5);
  const v4u w610 = LDS16(wb1 + 15360);
  MFV(a00_2, w604, (v4u{f0_10a0, f0_10a1, f0_10b0, f0_10b1}));
  WAITC(63, 5);
  const v4u w611 = LDS16(wb1 + 16384);
  MFV(a00_3, w605, (v4u{f0_10a0, f0_10a1, f0_10b0, f0_10b1}));
  WAITC(63, 5);
  const v4u w612 = LDS16(wb1 + 17408);
  W23 = ld16(ws, vo0 + 1024, so509, SC1);
  MFV(a00_4, w606, (v4u{f0_10a0, f0_10a1, f0_10b0, f0_10b1}));
  WAITC(63, 5);
  const v4u w613 = LDS16(wb1 + 18432);
  MFV(a00_5, w607, (v4u{f0_10a0, f0_10a1, f0_10b0, f0_10b1}));
  WAITC(63, 5);
  const v4u w614 = LDS16(wb1 + 19456);
  MFV(a00_6, w608, (v4u{f0_10a0, f0_10a1, f0_10b0, f0_10b1}));
  WAITC(63, 5);
  const v4u w615 = LDS16(wb1 + 20480);
  W24 = ld16(ws, vo0 + 2048, so509, SC1);
  MFV(sa0, ones, (v4u{f0_10a0, f0_10a1, f0_10b0, f0_10b1}));
  unsigned f0_12a0, f0_12a1; FIXA(f0_12, x00_12, mlo);
  MFV(sq0, (v4u{f0_10a0, f0_10a1, f0_10b0, f0_10b1}), (v4u{f0_10a0, f0_10a1, f0_10b0, f0_10b1}));
  unsigned f0_12b0, f0_12b1; FIXB(f0_12, x00_12, mhi);
  MFV(a00_0, w609, (v4u{f0_11a0, f0_11a1, f0_11b0, f0_11b1}));
  int so510; SMUL(so510, ko5, 5);
  const v4u w616 = LDS16(wb1 + 21504);
  int so511; SADDS(so511, w2p, so510);
  WAITC(63, 6);
  MFV(a00_1, w610, (v4u{f0_11a0, f0_11a1, f0_11b0, f0_11b1}));
  WAITC(63, 5);
  const v4u w617 = LDS16(wb1 + 22528);
  W25 = ld16(ws, vo0 + 0, so511, SC1);
  MFV(a00_2, w611, (v4u{f0_11a0, f0_11a1, f0_11b0, f0_11b1}));
  WAITC(63, 5);
  const v4u w618 = LDS16(wb1 + 23552);
  MFV(a00_3, w612, (v4u{f0_11a0, f0_11a1, f0_11b0, f0_11b1}));
  WAITC(63, 5);
  const v4u w619 = LDS16(wb1 + 24576);
  MFV(a00_4, w613, (v4u{f0_11a0, f0_11a1, f0_11b0, f0_11b1}));
  WAITC(63, 4);
  const v4u w620 = LDS16(wb1 + 25600);
  W26 = ld16(ws, vo0 + 1024, so511, SC1);
  MFV(a00_5, w614, (v4u{f0_11a0, f0_11a1, f0_11b0, f0_11b1}));
  int so512; SADDS(so512, w2q, so510);
  const v4u w621 = LDS16(wb1 + 26624);
  MFV(a00_6, w615, (v4u{f0_11a0, f0_11a1, f0_11b0, f0_11b1}));
  WAITC(63, 5);
  const v4u w622 = LDS16(wb1 + 27648);
  MFV(sa0, ones, (v4u{f0_11a0, f0_11a1, f0_11b0, f0_11b1}));
  WAITC(15, 15);
  unsigned f0_13a0, f0_13a1; FIXA(f0_13, x00_13, mlo);
  MFV(sq0, (v4u{f0_11a0, f0_11a1, f0_11b0, f0_11b1}), (v4u{f0_11a0, f0_11a1, f0_11b0, f0_11b1}));
  unsigned f0_13b0, f0_13b1; FIXB(f0_13, x00_13, mhi);
  MFV(a00_0, w616, (v4u{f0_12a0, f0_12a1, f0_12b0, f0_12b1}));
  WAITC(63, 5);
  const v4u w623 = LDS16(wb1 + 28672);
  W27 = ld16(ws, vo0 + 0, so512, SC1);
  MFV(a00_1, w617, (v4u{f0_12a0, f0_12a1, f0_12b0, f0_12b1}));
  WAITC(63, 5);
  const v4u w624 = LDS16(wb1 + 29696);
  MFV(a00_2, w618, (v4u{f0_12a0, f0_12a1, f0_12b0, f0_12b1}));
  WAITC(63, 5);
  const v4u w625 = LDS16(wb1 + 30720);
  MFV(a00_3, w619, (v4u{f0_12a0, f0_12a1, f0_12b0, f0_12b1}));
  WAITC(63, 5);
  const v4u w626 = LDS16(wb1 + 31744);
  W28 = ld16(ws, vo0 + 1024, so512, SC1);
  MFV(a00_4, w620, (v4u{f0_12a0, f0_12a1, f0_12b0, f0_12b1}));
  WAITC(63, 5);
  const v4u w627 = LDS16(wb1 + 32768);
  MFV(a00_5, w621, (v4u{f0_12a0, f0_12a1, f0_12b0, f0_12b1}));
  WAITC(63, 5);
  const v4u w628 = LDS16(wb1 + 33792);
  MFV(a00_6, w622, (v4u{f0_12a0, f0_12a1, f0_12b0, f0_12b1}));
  WAITC(63, 5);
  const v4u w629 = LDS16(wb1 + 34816);
  W29 = ld16(ws, vo0 + 2048, so512, SC1);
  MFV(sa0, ones, (v4u{f0_12a0, f0_12a1, f0_12b0, f0_12b1}));
  WAITC(17, 15);
  unsigned f0_14a0, f0_14a1; FIXA(f0_14, x00_14, mlo);
  MFV(sq0, (v4u{f0_12a0, f0_12a1, f0_12b0, f0_12b1}), (v4u{f0_12a0, f0_12a1, f0_12b0, f0_12b1}));
  unsigned f0_14b0, f0_14b1; FIXB(f0_14, x00_14, mhi);
  MFV(a00_0, w623, (v4u{f0_13a0, f0_13a1, f0_13b0, f0_13b1}));
  int so513; SMUL(so513, ko6, 5);
  const v4u w630 = LDS16(wb1 + 35840);
  int so514; SADDS(so514, w2p, so513);
  WAITC(63, 6);
  MFV(a00_1, w624, (v4u{f0_13a0, f0_13a1, f0_13b0, f0_13b1}));
  WAITC(63, 5);
  const v4u w631 = LDS16(wb1 + 36864);
  W30 = ld16(ws, vo0 + 0, so514, SC1);
  MFV(a00_2, w625, (v4u{f0_13a0, f0_13a1, f0_13b0, f0_13b1}));
  WAITC(63, 5);
  const v4u w632 = LDS16(wb1 + 37888);
  MFV(a00_3, w626, (v4u{f0_13a0, f0_13a1, f0_13b0, f0_13b1}));
  WAITC(63, 5);
  const v4u w633 = LDS16(wb1 + 38912);
  MFV(a00_4, w627, (v4u{f0_13a0, f0_13a1, f0_13b0, f0_13b1}));
  WAITC(63, 4);
  const v4u w634 = LDS16(wb1 + 39936);
  W31 = ld16(ws, vo0 + 1024, so514, SC1);
  MFV(a00_5, w628, (v4u{f0_13a0, f0_13a1, f0_13b0, f0_13b1}));
  int so515; SADDS(so515, w2q, so513);
  const v4u w635 = LDS16(wb1 + 40960);
  MFV(a00_6, w629, (v4u{f0_13a0, f0_13a1, f0_13b0, f0_13b1}));
  WAITC(63, 5);
  const v4u w636 = LDS16(wb1 + 41984);
  MFV(sa0, ones, (v4u{f0_13a0, f0_13a1, f0_13b0, f0_13b1}));
  WAITC(18, 15);
  unsigned f0_15a0, f0_15a1; FIXA(f0_15, x00_15, mlo);
  MFV(sq0, (v4u{f0_13a0, f0_13a1, f0_13b0, f0_13b1}), (v4u{f0_13a0, f0_13a1, f0_13b0, f0_13b1}));
  unsigned f0_15b0, f0_15b1; FIXB(f0_15, x00_15, mhi);
  MFV(a00_0, w630, (v4u{f0_14a0, f0_14a1, f0_14b0, f0_14b1}));
  WAITC(63, 5);
  const v4u w637 = LDS16(wb1 + 43008);
  W32 = ld16(ws, vo0 + 0, so515, SC1);
  MFV(a00_1, w631, (v4u{f0_14a0, f0_14a1, f0_14b0, f0_14b1}));
  WAITC(63, 5);
  const v4u w638 = LDS16(wb1 + 44032);
  MFV(a00_2, w632, (v4u{f0_14a0, f0_14a1, f0_14b0, f0_14b1}));
  WAITC(63, 5);
  const v4u w639 = LDS16(wb1 + 45056);
  MFV(a00_3, w633, (v4u{f0_14a0, f0_14a1, f0_14b0, f0_14b1}));
  WAITC(63, 5);
  const v4u w640 = LDS16(wb1 + 46080);
  W33 = ld16(ws, vo0 + 1024, so515, SC1);
  MFV(a00_4, w634, (v4u{f0_14a0, f0_14a1, f0_14b0, f0_14b1}));
  WAITC(63, 5);
  const v4u w641 = LDS16(wb1 + 47104);
  MFV(a00_5, w635, (v4u{f0_14a0, f0_14a1, f0_14b0, f0_14b1}));
  WAITC(63, 5);
  const v4u w642 = LDS16(wb1 + 48128);
  MFV(a00_6, w636, (v4u{f0_14a0, f0_14a1, f0_14b0, f0_14b1}));
  WAITC(63, 5);
  const v4u w643 = LDS16(wb1 + 49152);
  POLL_LD(pl488, vp8, (C_S1B) * 256);
  MFV(sa0, ones, (v4u{f0_14a0, f0_14a1, f0_14b0, f0_14b1}));
  WAITC(20, 15);
  unsigned f0_16a0, f0_16a1; FIXA(f0_16, x00_16, mlo);
  MFV(sq0, (v4u{f0_14a0, f0_14a1, f0_14b0, f0_14b1}), (v4u{f0_14a0, f0_14a1, f0_14b0, f0_14b1}));
  unsigned f0_16b0, f0_16b1; FIXB(f0_16, x00_16, mhi);
  MFV(a00_0, w637, (v4u{f0_15a0, f0_15a1, f0_15b0, f0_15b1}));
  WAITC(63, 4);
  const v4u w644 = LDS16(wb1 + 50176);
  W34 = ld16(ws, vo0 + 2048, so515, SC1);
  MFV(a00_1, w638, (v4u{f0_15a0, f0_15a1, f0_15b0, f0_15b1}));
  int so516; SMUL(so516, ko7, 5);
  const v4u w645 = LDS16(wb1 + 51200);
  int so517; SADDS(so517, w2p, so516);
  MFV(a00_2, w639, (v4u{f0_15a0, f0_15a1, f0_15b0, f0_15b1}));
  WAITC(63, 5);
  const v4u w646 = LDS16(wb1 + 52224);
  MFV(a00_3, w640, (v4u{f0_15a0, f0_15a1, f0_15b0, f0_15b1}));
  WAITC(63, 5);
  const v4u w647 = LDS16(wb1 + 53248);
  W35 = ld16(ws, vo0 + 0, so517, SC1);
  MFV(a00_4, w641, (v4u{f0_15a0, f0_15a1, f0_15b0, f0_15b1}));
  WAITC(63, 5);
  const v4u w648 = LDS16(wb1 + 54272);
  MFV(a00_5, w642, (v4u{f0_15a0, f0_15a1, f0_15b0, f0_15b1}));
  WAITC(63, 5);
  const v4u w649 = LDS16(wb1 + 55296);
  MFV(a00_6, w643, (v4u{f0_15a0, f0_15a1, f0_15b0, f0_15b1}));
  WAITC(63, 5);
  const v4u w650 = LDS16(wb1 + 56320);
  W36 = ld16(ws, vo0 + 1024, so517, SC1);
  MFV(sa0, ones, (v4u{f0_15a0, f0_15a1, f0_15b0, f0_15b1}));
  WAITC(22, 15);
  unsigned f0_17a0, f0_17a1; FIXA(f0_17, x00_17, mlo);
  MFV(sq0, (v4u{f0_15a0, f0_15a1, f0_15b0, f0_15b1}), (v4u{f0_15a0, f0_15a1, f0_15b0, f0_15b1}));
  unsigned f0_17b0, f0_17b1; FIXB(f0_17, x00_17, mhi);
  MFV(a00_0, w644, (v4u{f0_16a0, f0_16a1, f0_16b0, f0_16b1}));
  int so518; SADDS(so518, w2q, so516);
  const v4u w651 = LDS16(wb1 + 57344);
  W37 = ld16(ws, vo0 + 0, so518, SC1);
  WAITC(63, 6);
  MFV(a00_1, w645, (v4u{f0_16a0, f0_16a1, f0_16b0, f0_16b1}));
  WAITC(63, 5);
  const v4u w652 = LDS16(wb1 + 58368);
  MFV(a00_2, w646, (v4u{f0_16a0, f0_16a1, f0_16b0, f0_16b1}));
  WAITC(63, 5);
  const v4u w653 = LDS16(wb1 + 59392);
  MFV(a00_3, w647, (v4u{f0_16a0, f0_16a1, f0_16b0, f0_16b1}));
  WAITC(63, 5);
  const v4u w654 = LDS16(wb1 + 60416);
  W38 = ld16(ws, vo0 + 1024, so518, SC1);
  MFV(a00_4, w648, (v4u{f0_16a0, f0_16a1, f0_16b0, f0_16b1}));
  WAITC(63, 5);
  const v4u w655 = LDS16(wb1 + 61440);
  MFV(a00_5, w649, (v4u{f0_16a0, f0_16a1, f0_16b0, f0_16b1}));
  WAITC(63, 5);
  const v4u w656 = LDS16(wb1 + 62464);
  MFV(a00_6, w650, (v4u{f0_16a0, f0_16a1, f0_16b0, f0_16b1}));
  WAITC(63, 5);
  const v4u w657 = LDS16(wb1 + 63488);
  W39 = ld16(ws, vo0 + 2048, so518, SC1);
  MFV(sa0, ones, (v4u{f0_16a0, f0_16a1, f0_16b0, f0_16b1}));
  WAITC(24, 15);
  unsigned f0_18a0, f0_18a1; FIXA(f0_18, x00_18, mlo);
  MFV(sq0, (v4u{f0_16a0, f0_16a1, f0_16b0, f0_16b1}), (v4u{f0_16a0, f0_16a1, f0_16b0, f0_16b1}));
  unsigned f0_18b0, f0_18b1; FIXB(f0_18, x00_18, mhi);
  MFV(a00_0, w651, (v4u{f0_17a0, f0_17a1, f0_17b0, f0_17b1}));
  int so519; SMUL(so519, ko8, 5);
  const v4u w658 = LDS16(wb2 + 0);
  int so520; SADDS(so520, w2p, so519);
  WAITC(63, 6);
  MFV(a00_1, w652, (v4u{f0_17a0, f0_17a1, f0_17b0, f0_17b1}));
  WAITC(63, 5);
  const v4u w659 = LDS16(wb2 + 1024);
  W40 = ld16(ws, vo0 + 0, so520, SC1);
  MFV(a00_2, w653, (v4u{f0_17a0, f0_17a1, f0_17b0, f0_17b1}));
  WAITC(63, 5);
  const v4u w660 = LDS16(wb2 + 2048);
  MFV(a00_3, w654, (v4u{f0_17a0, f0_17a1, f0_17b0, f0_17b1}));
  WAITC(63, 5);
  const v4u w661 = LDS16(wb2 + 3072);
  MFV(a00_4, w655, (v4u{f0_17a0, f0_17a1, f0_17b0, f0_17b1}));
  WAITC(63, 4);
  const v4u w662 = LDS16(wb2 + 4096);
  W41 = ld16(ws, vo0 + 1024, so520, SC1);
  MFV(a00_5, w656, (v4u{f0_17a0, f0_17a1, f0_17b0, f0_17b1}));
  int so521; SADDS(so521, w2q, so519);
  const v4u w663 = LDS16(wb2 + 5120);
  MFV(a00_6, w657, (v4u{f0_17a0, f0_17a1, f0_17b0, f0_17b1}));
  WAITC(63, 5);
  const v4u w664 = LDS16(wb2 + 6144);
  MFV(sa0, ones, (v4u{f0_17a0, f0_17a1, f0_17b0, f0_17b1}));
  WAITC(25, 15);
  unsigned f0_19a0, f0_19a1; FIXA(f0_19, x00_19, mlo);
  MFV(sq0, (v4u{f0_17a0, f0_17a1, f0_17b0, f0_17b1}), (v4u{f0_17a0, f0_17a1, f0_17b0, f0_17b1}));
  unsigned f0_19b0, f0_19b1; FIXB(f0_19, x00_19, mhi);
  MFV(a00_0, w658, (v4u{f0_18a0, f0_18a1, f0_18b0, f0_18b1}));
  WAITC(63, 5);
  const v4u w665 = LDS16(wb2 + 7168);
  W42 = ld16(ws, vo0 + 0, so521, SC1);
  MFV(a00_1, w659, (v4u{f0_18a0, f0_18a1, f0_18b0, f0_18b1}));
  WAITC(63, 5);
  const v4u w666 = LDS16(wb2 + 8192);
  MFV(a00_2, w660, (v4u{f0_18a0, f0_18a1, f0_18b0, f0_18b1}));
  WAITC(63, 5);
  const v4u w667 = LDS16(wb2 + 9216);
  MFV(a00_3, w661, (v4u{f0_18a0, f0_18a1, f0_18b0, f0_18b1}));
  WAITC(63, 5);
  const v4u w668 = LDS16(wb2 + 10240);
  W43 = ld16(ws, vo0 + 1024, so521, SC1);
  MFV(a00_4, w662, (v4u{f0_18a0, f0_18a1, f0_18b0, f0_18b1}));
  WAITC(63, 5);
  const v4u w669 = LDS16(wb2 + 11264);
  MFV(a00_5, w663, (v4u{f0_18a0, f0_18a1, f0_18b0, f0_18b1}));
  WAITC(63, 5);
  const v4u w670 = LDS16(wb2 + 12288);
  MFV(a00_6, w664, (v4u{f0_18a0, f0_18a1, f0_18b0, f0_18b1}));
  const v4u w671 = LDS16(wb2 + 13312);
  W44 = ld16(ws, vo0 + 2048, so521, SC1);
  MFV(sa0, ones, (v4u{f0_18a0, f0_18a1, f0_18b0, f0_18b1}));
  WAITC(11, 6);
  POLL_CK(pl488, vp8, (C_S1B) * 256, 20);
  MFV(sq0, (v4u{f0_18a0, f0_18a1, f0_18b0, f0_18b1}), (v4u{f0_18a0, f0_18a1, f0_18b0, f0_18b1}));
  int so489; SADDS(so489, xb01, xo0);
  MFV(a00_0, w665, (v4u{f0_19a0, f0_19a1, f0_19b0, f0_19b1}));
  WAITC(63, 5);
  v4u x01_0 = ld16(ws, vo0 + 0, so489, SC1);
  MFV(a00_1, w666, (v4u{f0_19a0, f0_19a1, f0_19b0, f0_19b1}));
  WAITC(63, 4);
  MFV(a00_2, w667, (v4u{f0_19a0, f0_19a1, f0_19b0, f0_19b1}));
  WAITC(63, 3);
  MFV(a00_3, w668, (v4u{f0_19a0, f0_19a1, f0_19b0, f0_19b1}));
  WAITC(63, 2);
  v4u x01_1 = ld16(ws, vo0 + 1024, so489, SC1);
  MFV(a00_4, w669, (v4u{f0_19a0, f0_19a1, f0_19b0, f0_19b1}));
  WAITC(63, 1);
  MFV(a00_5, w670, (v4u{f0_19a0, f0_19a1, f0_19b0, f0_19b1}));
  WAITC(63, 0);
  MFV(a00_6, w671, (v4u{f0_19a0, f0_19a1, f0_19b0, f0_19b1}));
  v4u x01_2 = ld16(ws, vo0 + 2048, so489, SC1);
  MFV(sa0, ones, (v4u{f0_19a0, f0_19a1, f0_19b0, f0_19b1}));
  MFV(sq0, (v4u{f0_19a0, f0_19a1, f0_19b0, f0_19b1}), (v4u{f0_19a0, f0_19a1, f0_19b0, f0_19b1}));
  v4u x01_3 = ld16(ws, vo0 + 3072, so489, SC1);
  STAMP(9);
  const f32x4 cs672 = LDSF4(cbp + 0);
  const f32x4 bb679 = LDSF4(cbp + 64);
  const f32x4 cs673 = LDSF4(cbp + 128);
  const f32x4 bb680 = LDSF4(cbp + 192);
  NOP8(); TIE(sq0); TIE(sa0); TIE(a00_0); TIE(a00_1); TIE(a00_2); TIE(a00_3); TIE(a00_4); TIE(a00_5); TIE(a00_6);
  float rs00, nm00; { const float d_ = selq(sq0[0], sq0[1], sq0[2], sq0[3], lane); lnstat(sa0[0], xsum_g(isdiag ? d_ : 0.f), rs00, nm00); }
  const float rl00 = rs00 * 1.4426950408889634f, nl00 = nm00 * 1.4426950408889634f; const f32x2 r200 = {rl00, rl00}, n200 = {nl00, nl00}; SB();
  WAITC(63, 2);
  const f32x2 m686 = PFMA(LO2(cs672), n200, LO2(bb679)); SB();
  const f32x2 m698 = PFMA(HI2(cs672), n200, HI2(bb679)); SB();
  const f32x2 m688 = PFMA(LO2(a00_0), r200, m686); SB();
  const f32x2 m700 = PFMA(HI2(a00_0), r200, m698); SB();
  const float m689 = __builtin_amdgcn_exp2f(m688[0]); SB();
  int so490; SADDS(so490, xb01, xo1);
  v4u x01_4 = ld16(ws, vo0 + 0, so490, SC1);
  const float m690 = __builtin_amdgcn_exp2f(m688[1]); SB();
  const float m701 = __builtin_amdgcn_exp2f(m700[0]); SB();
  const float m702 = __builtin_amdgcn_exp2f(m700[1]); SB();
  v4u x01_5 = ld16(ws, vo0 + 1024, so490, SC1);
  const f32x4 cs674 = LDSF4(cbp + 256);
  const f32x4 bb681 = LDSF4(cbp + 320);
  const f32x2 m691 = kone + f32x2{m689, m690}; SB();
  const f32x2 m703 = kone + f32x2{m701, m702}; SB();
  const f32x2 m692 = PFMA(m691, m691, kone); SB();
  v4u x01_6 = ld16(ws, vo0 + 2048, so490, SC1);
  const f32x2 m704 = PFMA(m703, m703, kone); SB();
  const float m693 = __builtin_amdgcn_rcpf(m692[0]); SB();
  const float m694 = __builtin_amdgcn_rcpf(m692[1]); SB();
  v4u x01_7 = ld16(ws, vo0 + 3072, so490, SC1);
  const float m705 = __builtin_amdgcn_rcpf(m704[0]); SB();
  const float m706 = __builtin_amdgcn_rcpf(m704[1]); SB();
  const f32x2 m695 = PFMA((f32x2{m693, m694}), km2, kln2); SB();
  int so491; SADDS(so491, xb01, xo2);
  v4u x01_8 = ld16(ws, vo0 + 0, so491, SC1);
  const f32x2 m707 = PFMA((f32x2{m705, m706}), km2, kln2); SB();
  const f32x2 m696 = m688 * m695; SB();
  const f32x2 m708 = m700 * m707; SB();
  const unsigned m697 = pk(m696[0], m696[1]); SB();
  const unsigned m709 = pk(m708[0], m708[1]); SB();
  v4u x01_9 = ld16(ws, vo0 + 1024, so491, SC1);
  WAITC(63, 2);
  const f32x2 m710 = PFMA(LO2(cs673), n200, LO2(bb680)); SB();
  const f32x2 m722 = PFMA(HI2(cs673), n200, HI2(bb680)); SB();
  const f32x2 m712 = PFMA(LO2(a00_1), r200, m710); SB();
  const f32x2 m724 = PFMA(HI2(a00_1), r200, m722); SB();
  const float m713 = __builtin_amdgcn_exp2f(m712[0]); SB();
  const float m714 = __builtin_amdgcn_exp2f(m712[1]); SB();
  const float m725 = __builtin_amdgcn_exp2f(m724[0]); SB();
  const float m726 = __builtin_amdgcn_exp2f(m724[1]); SB();
  const f32x4 cs675 = LDSF4(cbp + 384);
  const f32x4 bb682 = LDSF4(cbp + 448);
  const f32x2 m715 = kone + f32x2{m713, m714}; SB();
  const f32x2 m727 = kone + f32x2{m725, m726}; SB();
  const f32x2 m716 = PFMA(m715, m715, kone); SB();
  const f32x2 m728 = PFMA(m727, m727, kone); SB();
  const float m717 = __builtin_amdgcn_rcpf(m716[0]); SB();
  const float m718 = __builtin_amdgcn_rcpf(m716[1]); SB();
  const float m729 = __builtin_amdgcn_rcpf(m728[0]); SB();
  const float m730 = __builtin_amdgcn_rcpf(m728[1]); SB();
  const f32x2 m719 = PFMA((f32x2{m717, m718}), km2, kln2); SB();
  const f32x2 m731 = PFMA((f32x2{m729, m730}), km2, kln2); SB();
  const f32x2 m720 = m712 * m719; SB();
  const f32x2 m732 = m724 * m731; SB();
  const unsigned m721 = pk(m720[0], m720[1]); SB();
  const unsigned m733 = pk(m732[0], m732[1]); SB();
  st16(v4u{m697, m709, m721, m733}, ws, vo0 + 0, hs00);
  WAITC(63, 2);
  const f32x2 m734 = PFMA(LO2(cs674), n200, LO2(bb681)); SB();
  const f32x2 m746 = PFMA(HI2(cs674), n200, HI2(bb681)); SB();
  const f32x2 m736 = PFMA(LO2(a00_2), r200, m734); SB();
  const f32x2 m748 = PFMA(HI2(a00_2), r200, m746); SB();
  const float m737 = __builtin_amdgcn_exp2f(m736[0]); SB();
  const float m738 = __builtin_amdgcn_exp2f(m736[1]); SB();
  const float m749 = __builtin_amdgcn_exp2f(m748[0]); SB();
  const float m750 = __builtin_amdgcn_exp2f(m748[1]); SB();
  const f32x4 cs676 = LDSF4(cbp + 512);
  const f32x4 bb683 = LDSF4(cbp + 576);
  const f32x2 m739 = kone + f32x2{m737, m738}; SB();
  const f32x2 m751 = kone + f32x2{m749, m750}; SB();
  const f32x2 m740 = PFMA(m739, m739, kone); SB();
  const f32x2 m752 = PFMA(m751, m751, kone); SB();
  const float m741 = __builtin_amdgcn_rcpf(m740[0]); SB();
  const float m742 = __builtin_amdgcn_rcpf(m740[1]); SB();
  const float m753 = __builtin_amdgcn_rcpf(m752[0]); SB();
  const float m754 = __builtin_amdgcn_rcpf(m752[1]); SB();
  const f32x2 m743 = PFMA((f32x2{m741, m742}), km2, kln2); SB();
  const f32x2 m755 = PFMA((f32x2{m753, m754}), km2, kln2); SB();
  const f32x2 m744 = m736 * m743; SB();
  const f32x2 m756 = m748 * m755; SB();
  const unsigned m745 = pk(m744[0], m744[1]); SB();
  const unsigned m757 = pk(m756[0], m756[1]); SB();
  WAITC(63, 2);
  const f32x2 m758 = PFMA(LO2(cs675), n200, LO2(bb682)); SB();
  const f32x2 m770 = PFMA(HI2(cs675), n200, HI2(bb682)); SB();
  const f32x2 m760 = PFMA(LO2(a00_3), r200, m758); SB();
  const f32x2 m772 = PFMA(HI2(a00_3), r200, m770); SB();
  const float m761 = __builtin_amdgcn_exp2f(m760[0]); SB();
  const float m762 = __builtin_amdgcn_exp2f(m760[1]); SB();
  const float m773 = __builtin_amdgcn_exp2f(m772[0]); SB();
  const float m774 = __builtin_amdgcn_exp2f(m772[1]); SB();
  const f32x4 cs677 = LDSF4(cbp + 640);
  const f32x4 bb684 = LDSF4(cbp + 704);
  const f32x2 m763 = kone + f32x2{m761, m762}; SB();
  const f32x2 m775 = kone + f32x2{m773, m774}; SB();
  const f32x2 m764 = PFMA(m763, m763, kone); SB();
  const f32x2 m776 = PFMA(m775, m775, kone); SB();
  const float m765 = __builtin_amdgcn_rcpf(m764[0]); SB();
  const float m766 = __builtin_amdgcn_rcpf(m764[1]); SB();
  const float m777 = __builtin_amdgcn_rcpf(m776[0]); SB();
  const float m778 = __builtin_amdgcn_rcpf(m776[1]); SB();
  const f32x2 m767 = PFMA((f32x2{m765, m766}), km2, kln2); SB();
  const f32x2 m779 = PFMA((f32x2{m777, m778}), km2, kln2); SB();
  const f32x2 m768 = m760 * m767; SB();
  const f32x2 m780 = m772 * m779; SB();
  const unsigned m769 = pk(m768[0], m768[1]); SB();
  const unsigned m781 = pk(m780[0], m780[1]); SB();
  st16(v4u{m745, m757, m769, m781}, ws, vo0 + 1024, hs00);
  WAITC(63, 2);
  const f32x2 m782 = PFMA(LO2(cs676), n200, LO2(bb683)); SB();
  const f32x2 m794 = PFMA(HI2(cs676), n200, HI2(bb683)); SB();
  const f32x2 m784 = PFMA(LO2(a00_4), r200, m782); SB();
  const f32x2 m796 = PFMA(HI2(a00_4), r200, m794); SB();
  const float m785 = __builtin_amdgcn_exp2f(m784[0]); SB();
  const float m786 = __builtin_amdgcn_exp2f(m784[1]); SB();
  const float m797 = __builtin_amdgcn_exp2f(m796[0]); SB();
  const float m798 = __builtin_amdgcn_exp2f(m796[1]); SB();
  const f32x4 cs678 = LDSF4(cbp + 768);
  const f32x4 bb685 = LDSF4(cbp + 832);
  const f32x2 m787 = kone + f32x2{m785, m786}; SB();
  const f32x2 m799 = kone + f32x2{m797, m798}; SB();
  const f32x2 m788 = PFMA(m787, m787, kone); SB();
  const f32x2 m800 = PFMA(m799, m799, kone); SB();
  const float m789 = __builtin_amdgcn_rcpf(m788[0]); SB();
  const float m790 = __builtin_amdgcn_rcpf(m788[1]); SB();
  const float m801 = __builtin_amdgcn_rcpf(m800[0]); SB();
  const float m802 = __builtin_amdgcn_rcpf(m800[1]); SB();
  const f32x2 m791 = PFMA((f32x2{m789, m790}), km2, kln2); SB();
  const f32x2 m803 = PFMA((f32x2{m801, m802}), km2, kln2); SB();
  const f32x2 m792 = m784 * m791; SB();
  const f32x2 m804 = m796 * m803; SB();
  const unsigned m793 = pk(m792[0], m792[1]); SB();
  const unsigned m805 = pk(m804[0], m804[1]); SB();
  WAITC(63, 2);
  const f32x2 m806 = PFMA(LO2(cs677), n200, LO2(bb684)); SB();
  const f32x2 m818 = PFMA(HI2(cs677), n200, HI2(bb684)); SB();
  const f32x2 m808 = PFMA(LO2(a00_5), r200, m806); SB();
  const f32x2 m820 = PFMA(HI2(a00_5), r200, m818); SB();
  const float m809 = __builtin_amdgcn_exp2f(m808[0]); SB();
  const float m810 = __builtin_amdgcn_exp2f(m808[1]); SB();
  const float m821 = __builtin_amdgcn_exp2f(m820[0]); SB();
  const float m822 = __builtin_amdgcn_exp2f(m820[1]); SB();
  const f32x2 m811 = kone + f32x2{m809, m810}; SB();
  const f32x2 m823 = kone + f32x2{m821, m822}; SB();
  const f32x2 m812 = PFMA(m811, m811, kone); SB();
  const f32x2 m824 = PFMA(m823, m823, kone); SB();
  const float m813 = __builtin_amdgcn_rcpf(m812[0]); SB();
  const float m814 = __builtin_amdgcn_rcpf(m812[1]); SB();
  const float m825 = __builtin_amdgcn_rcpf(m824[0]); SB();
  const float m826 = __builtin_amdgcn_rcpf(m824[1]); SB();
  const f32x2 m815 = PFMA((f32x2{m813, m814}), km2, kln2); SB();
  const f32x2 m827 = PFMA((f32x2{m825, m826}), km2, kln2); SB();
  const f32x2 m816 = m808 * m815; SB();
  const f32x2 m828 = m820 * m827; SB();
  const unsigned m817 = pk(m816[0], m816[1]); SB();
  const unsigned m829 = pk(m828[0], m828[1]); SB();
  st16(v4u{m793, m805, m817, m829}, ws, vo0 + 2048, hs00);
  WAITC(63, 0);
  const f32x2 m830 = PFMA(LO2(cs678), n200, LO2(bb685)); SB();
  const f32x2 m842 = PFMA(HI2(cs678), n200, HI2(bb685)); SB();
  const f32x2 m832 = PFMA(LO2(a00_6), r200, m830); SB();
  const f32x2 m844 = PFMA(HI2(a00_6), r200, m842); SB();
  const float m833 = __builtin_amdgcn_exp2f(m832[0]); SB();
  const float m834 = __builtin_amdgcn_exp2f(m832[1]); SB();
  const float m845 = __builtin_amdgcn_exp2f(m844[0]); SB();
  const v4u w883 = LDS16(wb0 + 0);
  const v4u w884 = LDS16(wb0 + 1024);
  const v4u w885 = LDS16(wb0 + 2048);
  const v4u w886 = LDS16(wb0 + 3072);
  const v4u w887 = LDS16(wb0 + 4096);
  const v4u w888 = LDS16(wb0 + 5120);
  const v4u w889 = LDS16(wb0 + 6144);
  const float m846 = __builtin_amdgcn_exp2f(m844[1]); SB();
  const f32x2 m835 = kone + f32x2{m833, m834}; SB();
  const f32x2 m847 = kone + f32x2{m845, m846}; SB();
  const f32x2 m836 = PFMA(m835, m835, kone); SB();
  const f32x2 m848 = PFMA(m847, m847, kone); SB();
  const float m837 = __builtin_amdgcn_rcpf(m836[0]); SB();
  const float m838 = __builtin_amdgcn_rcpf(m836[1]); SB();
  const float m849 = __builtin_amdgcn_rcpf(m848[0]); SB();
  const float m850 = __builtin_amdgcn_rcpf(m848[1]); SB();
  const f32x2 m839 = PFMA((f32x2{m837, m838}), km2, kln2); SB();
  const f32x2 m851 = PFMA((f32x2{m849, m850}), km2, kln2); SB();
  const f32x2 m840 = m832 * m839; SB();
  const f32x2 m852 = m844 * m851; SB();
  const unsigned m841 = pk(m840[0], m840[1]); SB();
  const unsigned m853 = pk(m852[0], m852[1]); SB();
  st8(v2u{m841, m853}, ws, vl8, ht00);
  STAMP(10);
  WAITC(13, 15);
  unsigned f1_0a0, f1_0a1; FIXA(f1_0, x01_0, mlo);
  unsigned f1_0b0, f1_0b1; FIXB(f1_0, x01_0, mhi);
  WAITC(12, 15);
  unsigned f1_1a0, f1_1a1; FIXA(f1_1, x01_1, mlo);
  unsigned f1_1b0, f1_1b1; FIXB(f1_1, x01_1, mhi);
  WAITC(63, 6);
  f32x4 a01_0; MFV0(a01_0, w883, (v4u{f1_0a0, f1_0a1, f1_0b0, f1_0b1}));
  WAITC(63, 5);
  const v4u w890 = LDS16(wb0 + 7168);
  v4u x01_10 = ld16(ws, vo0 + 2048, so491, SC1);
  f32x4 a01_1; MFV0(a01_1, w884, (v4u{f1_0a0, f1_0a1, f1_0b0, f1_0b1}));
  WAITC(63, 5);
  const v4u w891 = LDS16(wb0 + 8192);
  f32x4 a01_2; MFV0(a01_2, w885, (v4u{f1_0a0, f1_0a1, f1_0b0, f1_0b1}));
  WAITC(63, 5);
  const v4u w892 = LDS16(wb0 + 9216);
  f32x4 a01_3; MFV0(a01_3, w886, (v4u{f1_0a0, f1_0a1, f1_0b0, f1_0b1}));
  WAITC(63, 4);
  const v4u w893 = LDS16(wb0 + 10240);
  v4u x01_11 = ld16(ws, vo0 + 3072, so491, SC1);
  f32x4 a01_4; MFV0(a01_4, w887, (v4u{f1_0a0, f1_0a1, f1_0b0, f1_0b1}));
  int so492; SADDS(so492, xb01, xo3);
  const v4u w894 = LDS16(wb0 + 11264);
  f32x4 a01_5; MFV0(a01_5, w888, (v4u{f1_0a0, f1_0a1, f1_0b0, f1_0b1}));
  WAITC(63, 5);
  const v4u w895 = LDS16(wb0 + 12288);
  f32x4 a01_6; MFV0(a01_6, w889, (v4u{f1_0a0, f1_0a1, f1_0b0, f1_0b1}));
  WAITC(63, 5);
  const v4u w896 = LDS16(wb0 + 13312);
  v4u x01_12 = ld16(ws, vo0 + 0, so492, SC1);
  f32x4 sa1; MFV0(sa1, ones, (v4u{f1_0a0, f1_0a1, f1_0b0, f1_0b1}));
  WAITC(14, 15);
  unsigned f1_2a0, f1_2a1; FIXA(f1_2, x01_2, mlo);
  f32x4 sq1; MFV0(sq1, (v4u{f1_0a0, f1_0a1, f1_0b0, f1_0b1}), (v4u{f1_0a0, f1_0a1, f1_0b0, f1_0b1}));
  unsigned f1_2b0, f1_2b1; FIXB(f1_2, x01_2, mhi);
  MFV(a01_0, w890, (v4u{f1_1a0, f1_1a1, f1_1b0, f1_1b1}));
  WAITC(63, 5);
  const v4u w897 = LDS16(wb0 + 14336);
  v4u x01_13 = ld16(ws, vo0 + 1024, so492, SC1);
  MFV(a01_1, w891, (v4u{f1_1a0, f1_1a1, f1_1b0, f1_1b1}));
  WAITC(63, 5);
  const v4u w898 = LDS16(wb0 + 15360);
  MFV(a01_2, w892, (v4u{f1_1a0, f1_1a1, f1_1b0, f1_1b1}));
  WAITC(63, 5);
  const v4u w899 = LDS16(wb0 + 16384);
  MFV(a01_3, w893, (v4u{f1_1a0, f1_1a1, f1_1b0, f1_1b1}));
  WAITC(63, 5);
  const v4u w900 = LDS16(wb0 + 17408);
  v4u x01_14 = ld16(ws, vo0 + 2048, so492, SC1);
  MFV(a01_4, w894, (v4u{f1_1a0, f1_1a1, f1_1b0, f1_1b1}));
  WAITC(63, 5);
  const v4u w901 = LDS16(wb0 + 18432);
  MFV(a01_5, w895, (v4u{f1_1a0, f1_1a1, f1_1b0, f1_1b1}));
  WAITC(63, 5);
  const v4u w902 = LDS16(wb0 + 19456);
  MFV(a01_6, w896, (v4u{f1_1a0, f1_1a1, f1_1b0, f1_1b1}));
  WAITC(63, 5);
  const v4u w903 = LDS16(wb0 + 20480);
  v4u x01_15 = ld16(ws, vo0 + 3072, so492, SC1);
  MFV(sa1, ones, (v4u{f1_1a0, f1_1a1, f1_1b0, f1_1b1}));
  WAITC(16, 15);
  unsigned f1_3a0, f1_3a1; FIXA(f1_3, x01_3, mlo);
  MFV(sq1, (v4u{f1_1a0, f1_1a1, f1_1b0, f1_1b1}), (v4u{f1_1a0, f1_1a1, f1_1b0, f1_1b1}));
  unsigned f1_3b0, f1_3b1; FIXB(f1_3, x01_3, mhi);
  MFV(a01_0, w897, (v4u{f1_2a0, f1_2a1, f1_2b0, f1_2b1}));
  int so493; SADDS(so493, xb01, xo4);
  const v4u w904 = LDS16(wb0 + 21504);
  v4u x01_16 = ld16(ws, vo0 + 0, so493, SC1);
  WAITC(63, 6);
  MFV(a01_1, w898, (v4u{f1_2a0, f1_2a1, f1_2b0, f1_2b1}));
  WAITC(63, 5);
  const v4u w905 = LDS16(wb0 + 22528);
  MFV(a01_2, w899, (v4u{f1_2a0, f1_2a1, f1_2b0, f1_2b1}));
  WAITC(63, 5);
  const v4u w906 = LDS16(wb0 + 23552);
  MFV(a01_3, w900, (v4u{f1_2a0, f1_2a1, f1_2b0, f1_2b1}));
  WAITC(63, 5);
  const v4u w907 = LDS16(wb0 + 24576);
  v4u x01_17 = ld16(ws, vo0 + 1024, so493, SC1);
  MFV(a01_4, w901, (v4u{f1_2a0, f1_2a1, f1_2b0, f1_2b1}));
  WAITC(63, 5);
  const v4u w908 = LDS16(wb0 + 25600);
  MFV(a01_5, w902, (v4u{f1_2a0, f1_2a1, f1_2b0, f1_2b1}));
  WAITC(63, 5);
  const v4u w909 = LDS16(wb0 + 26624);
  MFV(a01_6, w903, (v4u{f1_2a0, f1_2a1, f1_2b0, f1_2b1}));
  WAITC(63, 5);
  const v4u w910 = LDS16(wb0 + 27648);
  v4u x01_18 = ld16(ws, vo0 + 2048, so493, SC1);
  MFV(sa1, ones, (v4u{f1_2a0, f1_2a1, f1_2b0, f1_2b1}));
  WAITC(18, 15);
  unsigned f1_4a0, f1_4a1; FIXA(f1_4, x01_4, mlo);
  MFV(sq1, (v4u{f1_2a0, f1_2a1, f1_2b0, f1_2b1}), (v4u{f1_2a0, f1_2a1, f1_2b0, f1_2b1}));
  unsigned f1_4b0, f1_4b1; FIXB(f1_4, x01_4, mhi);
  MFV(a01_0, w904, (v4u{f1_3a0, f1_3a1, f1_3b0, f1_3b1}));
  WAITC(63, 5);
  const v4u w911 = LDS16(wb0 + 28672);
  v4u x01_19 = ld16(ws, vo0 + 3072, so493, SC1);
  MFV(a01_1, w905, (v4u{f1_3a0, f1_3a1, f1_3b0, f1_3b1}));
  WAITC(63, 5);
  const v4u w912 = LDS16(wb0 + 29696);
  MFV(a01_2, w906, (v4u{f1_3a0, f1_3a1, f1_3b0, f1_3b1}));
  WAITC(63, 5);
  const v4u w913 = LDS16(wb0 + 30720);
  MFV(a01_3, w907, (v4u{f1_3a0, f1_3a1, f1_3b0, f1_3b1}));
  WAITC(63, 5);
  const v4u w914 = LDS16(wb0 + 31744);
  MFV(a01_4, w908, (v4u{f1_3a0, f1_3a1, f1_3b0, f1_3b1}));
  WAITC(63, 5);
  const v4u w915 = LDS16(wb0 + 32768);
  MFV(a01_5, w909, (v4u{f1_3a0, f1_3a1, f1_3b0, f1_3b1}));
  WAITC(63, 5);
  const v4u w916 = LDS16(wb0 + 33792);
  MFV(a01_6, w910, (v4u{f1_3a0, f1_3a1, f1_3b0, f1_3b1}));
  WAITC(63, 5);
  const v4u w917 = LDS16(wb0 + 34816);
  MFV(sa1, ones, (v4u{f1_3a0, f1_3a1, f1_3b0, f1_3b1}));
  WAITC(18, 15);
  unsigned f1_5a0, f1_5a1; FIXA(f1_5, x01_5, mlo);
  MFV(sq1, (v4u{f1_3a0, f1_3a1, f1_3b0, f1_3b1}), (v4u{f1_3a0, f1_3a1, f1_3b0, f1_3b1}));
  unsigned f1_5b0, f1_5b1; FIXB(f1_5, x01_5, mhi);
  MFV(a01_0, w911, (v4u{f1_4a0, f1_4a1, f1_4b0, f1_4b1}));
  WAITC(63, 5);
  const v4u w918 = LDS16(wb0 + 35840);
  MFV(a01_1, w912, (v4u{f1_4a0, f1_4a1, f1_4b0, f1_4b1}));
  WAITC(63, 5);
  const v4u w919 = LDS16(wb0 + 36864);
  MFV(a01_2, w913, (v4u{f1_4a0, f1_4a1, f1_4b0, f1_4b1}));
  WAITC(63, 5);
  const v4u w920 = LDS16(wb0 + 37888);
  MFV(a01_3, w914, (v4u{f1_4a0, f1_4a1, f1_4b0, f1_4b1}));
  WAITC(63, 5);
  const v4u w921 = LDS16(wb0 + 38912);
  MFV(a01_4, w915, (v4u{f1_4a0, f1_4a1, f1_4b0, f1_4b1}));
  WAITC(63, 5);
  const v4u w922 = LDS16(wb0 + 39936);
  MFV(a01_5, w916, (v4u{f1_4a0, f1_4a1, f1_4b0, f1_4b1}));
  WAITC(63, 5);
  const v4u w923 = LDS16(wb0 + 40960);
  MFV(a01_6, w917, (v4u{f1_4a0, f1_4a1, f1_4b0, f1_4b1}));
  WAITC(63, 5);
  const v4u w924 = LDS16(wb0 + 41984);
  MFV(sa1, ones, (v4u{f1_4a0, f1_4a1, f1_4b0, f1_4b1}));
  WAITC(17, 15);
  unsigned f1_6a0, f1_6a1; FIXA(f1_6, x01_6, mlo);
  MFV(sq1, (v4u{f1_4a0, f1_4a1, f1_4b0, f1_4b1}), (v4u{f1_4a0, f1_4a1, f1_4b0, f1_4b1}));
  unsigned f1_6b0, f1_6b1; FIXB(f1_6, x01_6, mhi);
  MFV(a01_0, w918, (v4u{f1_5a0, f1_5a1, f1_5b0, f1_5b1}));
  WAITC(63, 5);
  const v4u w925 = LDS16(wb0 + 43008);
  MFV(a01_1, w919, (v4u{f1_5a0, f1_5a1, f1_5b0, f1_5b1}));
  WAITC(63, 5);
  const v4u w926 = LDS16(wb0 + 44032);
  MFV(a01_2, w920, (v4u{f1_5a0, f1_5a1, f1_5b0, f1_5b1}));
  WAITC(63, 5);
  const v4u w927 = LDS16(wb0 + 45056);
  MFV(a01_3, w921, (v4u{f1_5a0, f1_5a1, f1_5b0, f1_5b1}));
  WAITC(63, 5);
  const v4u w928 = LDS16(wb0 + 46080);
  MFV(a01_4, w922, (v4u{f1_5a0, f1_5a1, f1_5b0, f1_5b1}));
  WAITC(63, 5);
  const v4u w929 = LDS16(wb0 + 47104);
  MFV(a01_5, w923, (v4u{f1_5a0, f1_5a1, f1_5b0, f1_5b1}));
  WAITC(63, 5);
  const v4u w930 = LDS16(wb0 + 48128);
  DRAINBAR(10);
  MFV(a01_6, w924, (v4u{f1_5a0, f1_5a1, f1_5b0, f1_5b1}));
  WAITC(63, 5);
  const v4u w931 = LDS16(wb0 + 49152);
  ARRIVE(va1, (gl) * 256);
  MFV(sa1, ones, (v4u{f1_5a0, f1_5a1, f1_5b0, f1_5b1}));
  unsigned f1_7a0, f1_7a1; FIXA(f1_7, x01_7, mlo);
  MFV(sq1, (v4u{f1_5a0, f1_5a1, f1_5b0, f1_5b1}), (v4u{f1_5a0, f1_5a1, f1_5b0, f1_5b1}));
  unsigned f1_7b0, f1_7b1; FIXB(f1_7, x01_7, mhi);
  MFV(a01_0, w925, (v4u{f1_6a0, f1_6a1, f1_6b0, f1_6b1}));
  int so522; SMUL(so522, ko9, 5);
  const v4u w932 = LDS16(wb0 + 50176);
  int so523; SADDS(so523, w2p, so522);
  WAITC(63, 6);
  MFV(a01_1, w926, (v4u{f1_6a0, f1_6a1, f1_6b0, f1_6b1}));
  WAITC(63, 5);
  const v4u w933 = LDS16(wb0 + 51200);
  W45 = ld16(ws, vo0 + 0, so523, SC1);
  MFV(a01_2, w927, (v4u{f1_6a0, f1_6a1, f1_6b0, f1_6b1}));
  WAITC(63, 5);
  const v4u w934 = LDS16(wb0 + 52224);
  MFV(a01_3, w928, (v4u{f1_6a0, f1_6a1, f1_6b0, f1_6b1}));
  WAITC(63, 5);
  const v4u w935 = LDS16(wb0 + 53248);
  MFV(a01_4, w929, (v4u{f1_6a0, f1_6a1, f1_6b0, f1_6b1}));
  WAITC(63, 4);
  const v4u w936 = LDS16(wb0 + 54272);
  W46 = ld16(ws, vo0 + 1024, so523, SC1);
  MFV(a01_5, w930, (v4u{f1_6a0, f1_6a1, f1_6b0, f1_6b1}));
  int so524; SADDS(so524, w2q, so522);
  const v4u w937 = LDS16(wb0 + 55296);
  MFV(a01_6, w931, (v4u{f1_6a0, f1_6a1, f1_6b0, f1_6b1}));
  WAITC(63, 5);
  const v4u w938 = LDS16(wb0 + 56320);
  MFV(sa1, ones, (v4u{f1_6a0, f1_6a1, f1_6b0, f1_6b1}));
  unsigned f1_8a0, f1_8a1; FIXA(f1_8, x01_8, mlo);
  MFV(sq1, (v4u{f1_6a0, f1_6a1, f1_6b0, f1_6b1}), (v4u{f1_6a0, f1_6a1, f1_6b0, f1_6b1}));
  unsigned f1_8b0, f1_8b1; FIXB(f1_8, x01_8, mhi);
  MFV(a01_0, w932, (v4u{f1_7a0, f1_7a1, f1_7b0, f1_7b1}));
  WAITC(63, 5);
  const v4u w939 = LDS16(wb0 + 57344);
  W47 = ld16(ws, vo0 + 0, so524, SC1);
  MFV(a01_1, w933, (v4u{f1_7a0, f1_7a1, f1_7b0, f1_7b1}));
  WAITC(63, 5);
  const v4u w940 = LDS16(wb0 + 58368);
  MFV(a01_2, w934, (v4u{f1_7a0, f1_7a1, f1_7b0, f1_7b1}));
  WAITC(63, 5);
  const v4u w941 = LDS16(wb0 + 59392);
  MFV(a01_3, w935, (v4u{f1_7a0, f1_7a1, f1_7b0, f1_7b1}));
  WAITC(63, 5);
  const v4u w942 = LDS16(wb0 + 60416);
  W48 = ld16(ws, vo0 + 1024, so524, SC1);
  MFV(a01_4, w936, (v4u{f1_7a0, f1_7a1, f1_7b0, f1_7b1}));
  WAITC(63, 5);
  const v4u w943 = LDS16(wb0 + 61440);
  MFV(a01_5, w937, (v4u{f1_7a0, f1_7a1, f1_7b0, f1_7b1}));
  WAITC(63, 5);
  const v4u w944 = LDS16(wb0 + 62464);
  MFV(a01_6, w938, (v4u{f1_7a0, f1_7a1, f1_7b0, f1_7b1}));
  WAITC(63, 5);
  const v4u w945 = LDS16(wb0 + 63488);
  W49 = ld16(ws, vo0 + 2048, so524, SC1);
  MFV(sa1, ones, (v4u{f1_7a0, f1_7a1, f1_7b0, f1_7b1}));
  unsigned f1_9a0, f1_9a1; FIXA(f1_9, x01_9, mlo);
  MFV(sq1, (v4u{f1_7a0, f1_7a1, f1_7b0, f1_7b1}), (v4u{f1_7a0, f1_7a1, f1_7b0, f1_7b1}));
  unsigned f1_9b0, f1_9b1; FIXB(f1_9, x01_9, mhi);
  MFV(a01_0, w939, (v4u{f1_8a0, f1_8a1, f1_8b0, f1_8b1}));
  int so525; SMUL(so525, ko10, 5);
  const v4u w946 = LDS16(wb1 + 0);
  int so526; SADDS(so526, w2p, so525);
  WAITC(63, 6);
  MFV(a01_1, w940, (v4u{f1_8a0, f1_8a1, f1_8b0, f1_8b1}));
  WAITC(63, 5);
  const v4u w947 = LDS16(wb1 + 1024);
  W50 = ld16(ws, vo0 + 0, so526, SC1);
  MFV(a01_2, w941, (v4u{f1_8a0, f1_8a1, f1_8b0, f1_8b1}));
  WAITC(63, 5);
  const v4u w948 = LDS16(wb1 + 2048);
  MFV(a01_3, w942, (v4u{f1_8a0, f1_8a1, f1_8b0, f1_8b1}));
  WAITC(63, 5);
  const v4u w949 = LDS16(wb1 + 3072);
  MFV(a01_4, w943, (v4u{f1_8a0, f1_8a1, f1_8b0, f1_8b1}));
  WAITC(63, 4);
  const v4u w950 = LDS16(wb1 + 4096);
  W51 = ld16(ws, vo0 + 1024, so526, SC1);
  MFV(a01_5, w944, (v4u{f1_8a0, f1_8a1, f1_8b0, f1_8b1}));
  int so527; SADDS(so527, w2q, so525);
  const v4u w951 = LDS16(wb1 + 5120);
  MFV(a01_6, w945, (v4u{f1_8a0, f1_8a1, f1_8b0, f1_8b1}));
  WAITC(63, 5);
  const v4u w952 = LDS16(wb1 + 6144);
  MFV(sa1, ones, (v4u{f1_8a0, f1_8a1, f1_8b0, f1_8b1}));
  WAITC(17, 15);
  unsigned f1_10a0, f1_10a1; FIXA(f1_10, x01_10, mlo);
  MFV(sq1, (v4u{f1_8a0, f1_8a1, f1_8b0, f1_8b1}), (v4u{f1_8a0, f1_8a1, f1_8b0, f1_8b1}));
  unsigned f1_10b0, f1_10b1; FIXB(f1_10, x01_10, mhi);
  MFV(a01_0, w946, (v4u{f1_9a0, f1_9a1, f1_9b0, f1_9b1}));
  WAITC(63, 5);
  const v4u w953 = LDS16(wb1 + 7168);
  W52 = ld16(ws, vo0 + 0, so527, SC1);
  MFV(a01_1, w947, (v4u{f1_9a0, f1_9a1, f1_9b0, f1_9b1}));
  WAITC(63, 5);
  const v4u w954 = LDS16(wb1 + 8192);
  MFV(a01_2, w948, (v4u{f1_9a0, f1_9a1, f1_9b0, f1_9b1}));
  WAITC(63, 5);
  const v4u w955 = LDS16(wb1 + 9216);
  MFV(a01_3, w949, (v4u{f1_9a0, f1_9a1, f1_9b0, f1_9b1}));
  WAITC(63, 5);
  const v4u w956 = LDS16(wb1 + 10240);
  W53 = ld16(ws, vo0 + 1024, so527, SC1);
  MFV(a01_4, w950, (v4u{f1_9a0, f1_9a1, f1_9b0, f1_9b1}));
  WAITC(63, 5);
  const v4u w957 = LDS16(wb1 + 11264);
  MFV(a01_5, w951, (v4u{f1_9a0, f1_9a1, f1_9b0, f1_9b1}));
  WAITC(63, 5);
  const v4u w958 = LDS16(wb1 + 12288);
  MFV(a01_6, w952, (v4u{f1_9a0, f1_9a1, f1_9b0, f1_9b1}));
  WAITC(63, 5);
  const v4u w959 = LDS16(wb1 + 13312);
  W54 = ld16(ws, vo0 + 2048, so527, SC1);
  MFV(sa1, ones, (v4u{f1_9a0, f1_9a1, f1_9b0, f1_9b1}));
  WAITC(19, 15);
  unsigned f1_11a0, f1_11a1; FIXA(f1_11, x01_11, mlo);
  MFV(sq1, (v4u{f1_9a0, f1_9a1, f1_9b0, f1_9b1}), (v4u{f1_9a0, f1_9a1, f1_9b0, f1_9b1}));
  unsigned f1_11b0, f1_11b1; FIXB(f1_11, x01_11, mhi);
  MFV(a01_0, w953, (v4u{f1_10a0, f1_10a1, f1_10b0, f1_10b1}));
  WAITC(63, 5);
  const v4u w960 = LDS16(wb1 + 14336);
  W55 = ld16(ws, vo0 + 0, w2pz, SC1);
  MFV(a01_1, w954, (v4u{f1_10a0, f1_10a1, f1_10b0, f1_10b1}));
  WAITC(63, 5);
  const v4u w961 = LDS16(wb1 + 15360);
  MFV(a01_2, w955, (v4u{f1_10a0, f1_10a1, f1_10b0, f1_10b1}));
  WAITC(63, 5);
  const v4u w962 = LDS16(wb1 + 16384);
  MFV(a01_3, w956, (v4u{f1_10a0, f1_10a1, f1_10b0, f1_10b1}));
  WAITC(63, 5);
  const v4u w963 = LDS16(wb1 + 17408);
  W56 = ld16(ws, vo0 + 1024, w2pz, SC1);
  MFV(a01_4, w957, (v4u{f1_10a0, f1_10a1, f1_10b0, f1_10b1}));
  WAITC(63, 5);
  const v4u w964 = LDS16(wb1 + 18432);
  MFV(a01_5, w958, (v4u{f1_10a0, f1_10a1, f1_10b0, f1_10b1}));
  WAITC(63, 5);
  const v4u w965 = LDS16(wb1 + 19456);
  MFV(a01_6, w959, (v4u{f1_10a0, f1_10a1, f1_10b0, f1_10b1}));
  WAITC(63, 5);
  const v4u w966 = LDS16(wb1 + 20480);
  W57 = ld16(ws, vo0 + 0, w2qz, SC1);
  MFV(sa1, ones, (v4u{f1_10a0, f1_10a1, f1_10b0, f1_10b1}));
  WAITC(21, 15);
  unsigned f1_12a0, f1_12a1; FIXA(f1_12, x01_12, mlo);
  MFV(sq1, (v4u{f1_10a0, f1_10a1, f1_10b0, f1_10b1}), (v4u{f1_10a0, f1_10a1, f1_10b0, f1_10b1}));
  unsigned f1_12b0, f1_12b1; FIXB(f1_12, x01_12, mhi);
  MFV(a01_0, w960, (v4u{f1_11a0, f1_11a1, f1_11b0, f1_11b1}));
  WAITC(63, 5);
  const v4u w967 = LDS16(wb1 + 21504);
  W58 = ld16(ws, vo0 + 1024, w2qz, SC1);
  MFV(a01_1, w961, (v4u{f1_11a0, f1_11a1, f1_11b0, f1_11b1}));
  WAITC(63, 5);
  const v4u w968 = LDS16(wb1 + 22528);
  MFV(a01_2, w962, (v4u{f1_11a0, f1_11a1, f1_11b0, f1_11b1}));
  WAITC(63, 5);
  const v4u w969 = LDS16(wb1 + 23552);
  MFV(a01_3, w963, (v4u{f1_11a0, f1_11a1, f1_11b0, f1_11b1}));
  WAITC(63, 4);
  const v4u w970 = LDS16(wb1 + 24576);
  W59 = ld16(ws, vo0 + 2048, w2qz, SC1);
  MFV(a01_4, w964, (v4u{f1_11a0, f1_11a1, f1_11b0, f1_11b1}));
  int so528; SADD(so528, w2p, 15360);
  const v4u w971 = LDS16(wb1 + 25600);
  MFV(a01_5, w965, (v4u{f1_11a0, f1_11a1, f1_11b0, f1_11b1}));
  WAITC(63, 5);
  const v4u w972 = LDS16(wb1 + 26624);
  MFV(a01_6, w966, (v4u{f1_11a0, f1_11a1, f1_11b0, f1_11b1}));
  WAITC(63, 5);
  const v4u w973 = LDS16(wb1 + 27648);
  W60 = ld16(ws, vo0 + 0, so528, SC1);
  MFV(sa1, ones, (v4u{f1_11a0, f1_11a1, f1_11b0, f1_11b1}));
  WAITC(23, 15);
  unsigned f1_13a0, f1_13a1; FIXA(f1_13, x01_13, mlo);
  MFV(sq1, (v4u{f1_11a0, f1_11a1, f1_11b0, f1_11b1}), (v4u{f1_11a0, f1_11a1, f1_11b0, f1_11b1}));
  unsigned f1_13b0, f1_13b1; FIXB(f1_13, x01_13, mhi);
  MFV(a01_0, w967, (v4u{f1_12a0, f1_12a1, f1_12b0, f1_12b1}));
  WAITC(63, 4);
  const v4u w974 = LDS16(wb1 + 28672);
  W61 = ld16(ws, vo0 + 1024, so528, SC1);
  MFV(a01_1, w968, (v4u{f1_12a0, f1_12a1, f1_12b0, f1_12b1}));
  int so529; SADD(so529, w2q, 15360);
  const v4u w975 = LDS16(wb1 + 29696);
  MFV(a01_2, w969, (v4u{f1_12a0, f1_12a1, f1_12b0, f1_12b1}));
  WAITC(63, 5);
  const v4u w976 = LDS16(wb1 + 30720);
  MFV(a01_3, w970, (v4u{f1_12a0, f1_12a1, f1_12b0, f1_12b1}));
  WAITC(63, 5);
  const v4u w977 = LDS16(wb1 + 31744);
  W62 = ld16(ws, vo0 + 0, so529, SC1);
  MFV(a01_4, w971, (v4u{f1_12a0, f1_12a1, f1_12b0, f1_12b1}));
  WAITC(63, 5);
  const v4u w978 = LDS16(wb1 + 32768);
  MFV(a01_5, w972, (v4u{f1_12a0, f1_12a1, f1_12b0, f1_12b1}));
  WAITC(63, 5);
  const v4u w979 = LDS16(wb1 + 33792);
  MFV(a01_6, w973, (v4u{f1_12a0, f1_12a1, f1_12b0, f1_12b1}));
  WAITC(63, 5);
  const v4u w980 = LDS16(wb1 + 34816);
  W63 = ld16(ws, vo0 + 1024, so529, SC1);
  MFV(sa1, ones, (v4u{f1_12a0, f1_12a1, f1_12b0, f1_12b1}));
  WAITC(25, 15);
  unsigned f1_14a0, f1_14a1; FIXA(f1_14, x01_14, mlo);
  MFV(sq1, (v4u{f1_12a0, f1_12a1, f1_12b0, f1_12b1}), (v4u{f1_12a0, f1_12a1, f1_12b0, f1_12b1}));
  unsigned f1_14b0, f1_14b1; FIXB(f1_14, x01_14, mhi);
  MFV(a01_0, w974, (v4u{f1_13a0, f1_13a1, f1_13b0, f1_13b1}));
  WAITC(63, 5);
  const v4u w981 = LDS16(wb1 + 35840);
  POLL_LD(pl854, vp1, (gl2) * 256);
  MFV(a01_1, w975, (v4u{f1_13a0, f1_13a1, f1_13b0, f1_13b1}));
  WAITC(63, 5);
  const v4u w982 = LDS16(wb1 + 36864);
  MFV(a01_2, w976, (v4u{f1_13a0, f1_13a1, f1_13b0, f1_13b1}));
  WAITC(63, 5);
  const v4u w983 = LDS16(wb1 + 37888);
  MFV(a01_3, w977, (v4u{f1_13a0, f1_13a1, f1_13b0, f1_13b1}));
  WAITC(63, 4);
  const v4u w984 = LDS16(wb1 + 38912);
  W64 = ld16(ws, vo0 + 2048, so529, SC1);
  MFV(a01_4, w978, (v4u{f1_13a0, f1_13a1, f1_13b0, f1_13b1}));
  int so530; SADD(so530, w2p, 51200);
  const v4u w985 = LDS16(wb1 + 39936);
  MFV(a01_5, w979, (v4u{f1_13a0, f1_13a1, f1_13b0, f1_13b1}));
  WAITC(63, 5);
  const v4u w986 = LDS16(wb1 + 40960);
  MFV(a01_6, w980, (v4u{f1_13a0, f1_13a1, f1_13b0, f1_13b1}));
  WAITC(63, 5);
  const v4u w987 = LDS16(wb1 + 41984);
  W65 = ld16(ws, vo0 + 0, so530, SC1);
  MFV(sa1, ones, (v4u{f1_13a0, f1_13a1, f1_13b0, f1_13b1}));
  WAITC(27, 15);
  unsigned f1_15a0, f1_15a1; FIXA(f1_15, x01_15, mlo);
  MFV(sq1, (v4u{f1_13a0, f1_13a1, f1_13b0, f1_13b1}), (v4u{f1_13a0, f1_13a1, f1_13b0, f1_13b1}));
  unsigned f1_15b0, f1_15b1; FIXB(f1_15, x01_15, mhi);
  MFV(a01_0, w981, (v4u{f1_14a0, f1_14a1, f1_14b0, f1_14b1}));
  WAITC(63, 4);
  const v4u w988 = LDS16(wb1 + 43008);
  W66 = ld16(ws, vo0 + 1024, so530, SC1);
  MFV(a01_1, w982, (v4u{f1_14a0, f1_14a1, f1_14b0, f1_14b1}));
  int so531; SADD(so531, w2q, 51200);
  const v4u w989 = LDS16(wb1 + 44032);
  MFV(a01_2, w983, (v4u{f1_14a0, f1_14a1, f1_14b0, f1_14b1}));
  WAITC(63, 5);
  const v4u w990 = LDS16(wb1 + 45056);
  MFV(a01_3, w984, (v4u{f1_14a0, f1_14a1, f1_14b0, f1_14b1}));
  WAITC(63, 5);
  const v4u w991 = LDS16(wb1 + 46080);
  W67 = ld16(ws, vo0 + 0, so531, SC1);
  MFV(a01_4, w985, (v4u{f1_14a0, f1_14a1, f1_14b0, f1_14b1}));
  WAITC(63, 5);
  const v4u w992 = LDS16(wb1 + 47104);
  MFV(a01_5, w986, (v4u{f1_14a0, f1_14a1, f1_14b0, f1_14b1}));
  WAITC(63, 5);
  const v4u w993 = LDS16(wb1 + 48128);
  MFV(a01_6, w987, (v4u{f1_14a0, f1_14a1, f1_14b0, f1_14b1}));
  WAITC(63, 5);
  const v4u w994 = LDS16(wb1 + 49152);
  W68 = ld16(ws, vo0 + 1024, so531, SC1);
  MFV(sa1, ones, (v4u{f1_14a0, f1_14a1, f1_14b0, f1_14b1}));
  WAITC(29, 15);
  unsigned f1_16a0, f1_16a1; FIXA(f1_16, x01_16, mlo);
  MFV(sq1, (v4u{f1_14a0, f1_14a1, f1_14b0, f1_14b1}), (v4u{f1_14a0, f1_14a1, f1_14b0, f1_14b1}));
  unsigned f1_16b0, f1_16b1; FIXB(f1_16, x01_16, mhi);
  MFV(a01_0, w988, (v4u{f1_15a0, f1_15a1, f1_15b0, f1_15b1}));
  WAITC(63, 5);
  const v4u w995 = LDS16(wb1 + 50176);
  W69 = ld16(ws, vo0 + 2048, so531, SC1);
  MFV(a01_1, w989, (v4u{f1_15a0, f1_15a1, f1_15b0, f1_15b1}));
  WAITC(63, 5);
  const v4u w996 = LDS16(wb1 + 51200);
  MFV(a01_2, w990, (v4u{f1_15a0, f1_15a1, f1_15b0, f1_15b1}));
  WAITC(63, 5);
  const v4u w997 = LDS16(wb1 + 52224);
  MFV(a01_3, w991, (v4u{f1_15a0, f1_15a1, f1_15b0, f1_15b1}));
  WAITC(63, 5);
  const v4u w998 = LDS16(wb1 + 53248);
  const v2u e10_0 = ld8(ws, vxe + 0, ex0, SC1);
  MFV(a01_4, w992, (v4u{f1_15a0, f1_15a1, f1_15b0, f1_15b1}));
  WAITC(63, 5);
  const v4u w999 = LDS16(wb1 + 54272);
  MFV(a01_5, w993, (v4u{f1_15a0, f1_15a1, f1_15b0, f1_15b1}));
  WAITC(63, 5);
  const v4u w1000 = LDS16(wb1 + 55296);
  MFV(a01_6, w994, (v4u{f1_15a0, f1_15a1, f1_15b0, f1_15b1}));
  WAITC(63, 5);
  const v4u w1001 = LDS16(wb1 + 56320);
  const v2u ey0_0 = ld8(ws, vxe + 0, ey0, SC1);
  MFV(sa1, ones, (v4u{f1_15a0, f1_15a1, f1_15b0, f1_15b1}));
  WAITC(31, 15);
  unsigned f1_17a0, f1_17a1; FIXA(f1_17, x01_17, mlo);
  MFV(sq1, (v4u{f1_15a0, f1_15a1, f1_15b0, f1_15b1}), (v4u{f1_15a0, f1_15a1, f1_15b0, f1_15b1}));
  unsigned f1_17b0, f1_17b1; FIXB(f1_17, x01_17, mhi);
  MFV(a01_0, w995, (v4u{f1_16a0, f1_16a1, f1_16b0, f1_16b1}));
  WAITC(63, 5);
  const v4u w1002 = LDS16(wb1 + 57344);
  const v2u e10_1 = ld8(ws, vxe + 512, ex0, SC1);
  MFV(a01_1, w996, (v4u{f1_16a0, f1_16a1, f1_16b0, f1_16b1}));
  WAITC(63, 5);
  const v4u w1003 = LDS16(wb1 + 58368);
  MFV(a01_2, w997, (v4u{f1_16a0, f1_16a1, f1_16b0, f1_16b1}));
  WAITC(63, 5);
  const v4u w1004 = LDS16(wb1 + 59392);
  MFV(a01_3, w998, (v4u{f1_16a0, f1_16a1, f1_16b0, f1_16b1}));
  WAITC(63, 5);
  const v4u w1005 = LDS16(wb1 + 60416);
  const v2u ey0_1 = ld8(ws, vxe + 512, ey0, SC1);
  MFV(a01_4, w999, (v4u{f1_16a0, f1_16a1, f1_16b0, f1_16b1}));
  WAITC(63, 5);
  const v4u w1006 = LDS16(wb1 + 61440);
  MFV(a01_5, w1000, (v4u{f1_16a0, f1_16a1, f1_16b0, f1_16b1}));
  WAITC(63, 5);
  const v4u w1007 = LDS16(wb1 + 62464);
  MFV(a01_6, w1001, (v4u{f1_16a0, f1_16a1, f1_16b0, f1_16b1}));
  WAITC(63, 5);
  const v4u w1008 = LDS16(wb1 + 63488);
  const v2u e10_2 = ld8(ws, vxe + 0, ex30, SC1);
  MFV(sa1, ones, (v4u{f1_16a0, f1_16a1, f1_16b0, f1_16b1}));
  WAITC(33, 15);
  unsigned f1_18a0, f1_18a1; FIXA(f1_18, x01_18, mlo);
  MFV(sq1, (v4u{f1_16a0, f1_16a1, f1_16b0, f1_16b1}), (v4u{f1_16a0, f1_16a1, f1_16b0, f1_16b1}));
  unsigned f1_18b0, f1_18b1; FIXB(f1_18, x01_18, mhi);
  MFV(a01_0, w1002, (v4u{f1_17a0, f1_17a1, f1_17b0, f1_17b1}));
  WAITC(63, 5);
  const v4u w1009 = LDS16(wb2 + 0);
  const v2u ey0_2 = ld8(ws, vxe + 0, ey30, SC1);
  MFV(a01_1, w1003, (v4u{f1_17a0, f1_17a1, f1_17b0, f1_17b1}));
  WAITC(12, 4);
  const v4u w1010 = LDS16(wb2 + 1024);
  POLL_CK(pl854, vp1, (gl2) * 256, 16);
  MFV(a01_2, w1004, (v4u{f1_17a0, f1_17a1, f1_17b0, f1_17b1}));
  int so855; SADDS(so855, hb000, ko0);
  const v4u w1011 = LDS16(wb2 + 2048);
  MFV(a01_3, w1005, (v4u{f1_17a0, f1_17a1, f1_17b0, f1_17b1}));
  WAITC(63, 4);
  const v4u w1012 = LDS16(wb2 + 3072);
  v4u h000_0 = ld16(ws, vo0 + 0, so855, SC1);
  MFV(a01_4, w1006, (v4u{f1_17a0, f1_17a1, f1_17b0, f1_17b1}));
  int so856; SADDS(so856, hb000, ko1);
  const v4u w1013 = LDS16(wb2 + 4096);
  MFV(a01_5, w1007, (v4u{f1_17a0, f1_17a1, f1_17b0, f1_17b1}));
  WAITC(63, 5);
  const v4u w1014 = LDS16(wb2 + 5120);
  MFV(a01_6, w1008, (v4u{f1_17a0, f1_17a1, f1_17b0, f1_17b1}));
  WAITC(63, 5);
  const v4u w1015 = LDS16(wb2 + 6144);
  v4u h000_1 = ld16(ws, vo0 + 0, so856, SC1);
  MFV(sa1, ones, (v4u{f1_17a0, f1_17a1, f1_17b0, f1_17b1}));
  unsigned f1_19a0, f1_19a1; FIXA(f1_19, x01_19, mlo);
  MFV(sq1, (v4u{f1_17a0, f1_17a1, f1_17b0, f1_17b1}), (v4u{f1_17a0, f1_17a1, f1_17b0, f1_17b1}));
  unsigned f1_19b0, f1_19b1; FIXB(f1_19, x01_19, mhi);
  MFV(a01_0, w1009, (v4u{f1_18a0, f1_18a1, f1_18b0, f1_18b1}));
  int so857; SADDS(so857, hb000, ko2);
  const v4u w1016 = LDS16(wb2 + 7168);
  v4u h000_2 = ld16(ws, vo0 + 0, so857, SC1);
  WAITC(63, 6);
  MFV(a01_1, w1010, (v4u{f1_18a0, f1_18a1, f1_18b0, f1_18b1}));
  int so858; SADDS(so858, hb000, ko3);
  const v4u w1017 = LDS16(wb2 + 8192);
  WAITC(63, 6);
  MFV(a01_2, w1011, (v4u{f1_18a0, f1_18a1, f1_18b0, f1_18b1}));
  WAITC(63, 5);
  const v4u w1018 = LDS16(wb2 + 9216);
  MFV(a01_3, w1012, (v4u{f1_18a0, f1_18a1, f1_18b0, f1_18b1}));
  WAITC(63, 4);
  const v4u w1019 = LDS16(wb2 + 10240);
  v4u h000_3 = ld16(ws, vo0 + 0, so858, SC1);
  MFV(a01_4, w1013, (v4u{f1_18a0, f1_18a1, f1_18b0, f1_18b1}));
  int so859; SADDS(so859, hb000, ko4);
  const v4u w1020 = LDS16(wb2 + 11264);
  MFV(a01_5, w1014, (v4u{f1_18a0, f1_18a1, f1_18b0, f1_18b1}));
  WAITC(63, 5);
  const v4u w1021 = LDS16(wb2 + 12288);
  MFV(a01_6, w1015, (v4u{f1_18a0, f1_18a1, f1_18b0, f1_18b1}));
  const v4u w1022 = LDS16(wb2 + 13312);
  v4u h000_4 = ld16(ws, vo0 + 0, so859, SC1);
  MFV(sa1, ones, (v4u{f1_18a0, f1_18a1, f1_18b0, f1_18b1}));
  int so860; SADDS(so860, hb000, ko5);
  MFV(sq1, (v4u{f1_18a0, f1_18a1, f1_18b0, f1_18b1}), (v4u{f1_18a0, f1_18a1, f1_18b0, f1_18b1}));
  WAITC(63, 6);
  MFV(a01_0, w1016, (v4u{f1_19a0, f1_19a1, f1_19b0, f1_19b1}));
  WAITC(63, 5);
  v4u h000_5 = ld16(ws, vo0 + 0, so860, SC1);
  MFV(a01_1, w1017, (v4u{f1_19a0, f1_19a1, f1_19b0, f1_19b1}));
  WAITC(63, 4);
  MFV(a01_2, w1018, (v4u{f1_19a0, f1_19a1, f1_19b0, f1_19b1}));
  WAITC(63, 3);
  MFV(a01_3, w1019, (v4u{f1_19a0, f1_19a1, f1_19b0, f1_19b1}));
  WAITC(63, 2);
  MFV(a01_4, w1020, (v4u{f1_19a0, f1_19a1, f1_19b0, f1_19b1}));
  WAITC(63, 1);
  MFV(a01_5, w1021, (v4u{f1_19a0, f1_19a1, f1_19b0, f1_19b1}));
  WAITC(63, 0);
  MFV(a01_6, w1022, (v4u{f1_19a0, f1_19a1, f1_19b0, f1_19b1}));
  MFV(sa1, ones, (v4u{f1_19a0, f1_19a1, f1_19b0, f1_19b1}));
  MFV(sq1, (v4u{f1_19a0, f1_19a1, f1_19b0, f1_19b1}), (v4u{f1_19a0, f1_19a1, f1_19b0, f1_19b1}));
  STAMP(11);
  const f32x4 cs1023 = LDSF4(cbp + 0);
  const f32x4 bb1030 = LDSF4(cbp + 64);
  const f32x4 cs1024 = LDSF4(cbp + 128);
  const f32x4 bb1031 = LDSF4(cbp + 192);
  NOP8(); TIE(sq1); TIE(sa1); TIE(a01_0); TIE(a01_1); TIE(a01_2); TIE(a01_3); TIE(a01_4); TIE(a01_5); TIE(a01_6);
  float rs01, nm01; { const float d_ = selq(sq1[0], sq1[1], sq1[2], sq1[3], lane); lnstat(sa1[0], xsum_g(isdiag ? d_ : 0.f), rs01, nm01); }
  const float rl01 = rs01 * 1.4426950408889634f, nl01 = nm01 * 1.4426950408889634f; const f32x2 r201 = {rl01, rl01}, n201 = {nl01, nl01}; SB();
  WAITC(63, 2);
  const f32x2 m1037 = PFMA(LO2(cs1023), n201, LO2(bb1030)); SB();
  const f32x2 m1049 = PFMA(HI2(cs1023), n201, HI2(bb1030)); SB();
  const f32x2 m1039 = PFMA(LO2(a01_0), r201, m1037); SB();
  const f32x2 m1051 = PFMA(HI2(a01_0), r201, m1049); SB();
  const float m1040 = __builtin_amdgcn_exp2f(m1039[0]); SB();
  int so861; SADDS(so861, hb000, ko6);
  v4u h000_6 = ld16(ws, vo0 + 0, so861, SC1);
  const float m1041 = __builtin_amdgcn_exp2f(m1039[1]); SB();
  const float m1052 = __builtin_amdgcn_exp2f(m1051[0]); SB();
  const float m1053 = __builtin_amdgcn_exp2f(m1051[1]); SB();
  int so862; SADDS(so862, hb000, ko7);
  v4u h000_7 = ld16(ws, vo0 + 0, so862, SC1);
  const f32x4 cs1025 = LDSF4(cbp + 256);
  const f32x4 bb1032 = LDSF4(cbp + 320);
  const f32x2 m1042 = kone + f32x2{m1040, m1041}; SB();
  const f32x2 m1054 = kone + f32x2{m1052, m1053}; SB();
  const f32x2 m1043 = PFMA(m1042, m1042, kone); SB();
  int so863; SADDS(so863, hb000, ko8);
  v4u h000_8 = ld16(ws, vo0 + 0, so863, SC1);
  const f32x2 m1055 = PFMA(m1054, m1054, kone); SB();
  const float m1044 = __builtin_amdgcn_rcpf(m1043[0]); SB();
  const float m1045 = __builtin_amdgcn_rcpf(m1043[1]); SB();
  int so864; SADDS(so864, hb000, ko9);
  v4u h000_9 = ld16(ws, vo0 + 0, so864, SC1);
  const float m1056 = __builtin_amdgcn_rcpf(m1055[0]); SB();
  const float m1057 = __builtin_amdgcn_rcpf(m1055[1]); SB();
  const f32x2 m1046 = PFMA((f32x2{m1044, m1045}), km2, kln2); SB();
  int so865; SADDS(so865, hb000, ko10);
  v4u h000_10 = ld16(ws, vo0 + 0, so865, SC1);
  const f32x2 m1058 = PFMA((f32x2{m1056, m1057}), km2, kln2); SB();
  const f32x2 m1047 = m1039 * m1046; SB();
  const f32x2 m1059 = m1051 * m1058; SB();
  const unsigned m1048 = pk(m1047[0], m1047[1]); SB();
  const unsigned m1060 = pk(m1059[0], m1059[1]); SB();
  WAITC(63, 2);
  const f32x2 m1061 = PFMA(LO2(cs1024), n201, LO2(bb1031)); SB();
  const f32x2 m1073 = PFMA(HI2(cs1024), n201, HI2(bb1031)); SB();
  const f32x2 m1063 = PFMA(LO2(a01_1), r201, m1061); SB();
  const f32x2 m1075 = PFMA(HI2(a01_1), r201, m1073); SB();
  const float m1064 = __builtin_amdgcn_exp2f(m1063[0]); SB();
  const float m1065 = __builtin_amdgcn_exp2f(m1063[1]); SB();
  const float m1076 = __builtin_amdgcn_exp2f(m1075[0]); SB();
  const float m1077 = __builtin_amdgcn_exp2f(m1075[1]); SB();
  const f32x4 cs1026 = LDSF4(cbp + 384);
  const f32x4 bb1033 = LDSF4(cbp + 448);
  const f32x2 m1066 = kone + f32x2{m1064, m1065}; SB();
  const f32x2 m1078 = kone + f32x2{m1076, m1077}; SB();
  const f32x2 m1067 = PFMA(m1066, m1066, kone); SB();
  const f32x2 m1079 = PFMA(m1078, m1078, kone); SB();
  const float m1068 = __builtin_amdgcn_rcpf(m1067[0]); SB();
  const float m1069 = __builtin_amdgcn_rcpf(m1067[1]); SB();
  const float m1080 = __builtin_amdgcn_rcpf(m1079[0]); SB();
  const float m1081 = __builtin_amdgcn_rcpf(m1079[1]); SB();
  const f32x2 m1070 = PFMA((f32x2{m1068, m1069}), km2, kln2); SB();
  const f32x2 m1082 = PFMA((f32x2{m1080, m1081}), km2, kln2); SB();
  const f32x2 m1071 = m1063 * m1070; SB();
  const f32x2 m1083 = m1075 * m1082; SB();
  const unsigned m1072 = pk(m1071[0], m1071[1]); SB();
  const unsigned m1084 = pk(m1083[0], m1083[1]); SB();
  st16(v4u{m1048, m1060, m1072, m1084}, ws, vo0 + 0, hs01);
  WAITC(63, 2);
  const f32x2 m1085 = PFMA(LO2(cs1025), n201, LO2(bb1032)); SB();
  const f32x2 m1097 = PFMA(HI2(cs1025), n201, HI2(bb1032)); SB();
  const f32x2 m1087 = PFMA(LO2(a01_2), r201, m1085); SB();
  const f32x2 m1099 = PFMA(HI2(a01_2), r201, m1097); SB();
  const float m1088 = __builtin_amdgcn_exp2f(m1087[0]); SB();
  const float m1089 = __builtin_amdgcn_exp2f(m1087[1]); SB();
  const float m1100 = __builtin_amdgcn_exp2f(m1099[0]); SB();
  const float m1101 = __builtin_amdgcn_exp2f(m1099[1]); SB();
  const f32x4 cs1027 = LDSF4(cbp + 512);
  const f32x4 bb1034 = LDSF4(cbp + 576);
  const f32x2 m1090 = kone + f32x2{m1088, m1089}; SB();
  const f32x2 m1102 = kone + f32x2{m1100, m1101}; SB();
  const f32x2 m1091 = PFMA(m1090, m1090, kone); SB();
  const f32x2 m1103 = PFMA(m1102, m1102, kone); SB();
  const float m1092 = __builtin_amdgcn_rcpf(m1091[0]); SB();
  const float m1093 = __builtin_amdgcn_rcpf(m1091[1]); SB();
  const float m1104 = __builtin_amdgcn_rcpf(m1103[0]); SB();
  const float m1105 = __builtin_amdgcn_rcpf(m1103[1]); SB();
  const f32x2 m1094 = PFMA((f32x2{m1092, m1093}), km2, kln2); SB();
  const f32x2 m1106 = PFMA((f32x2{m1104, m1105}), km2, kln2); SB();
  const f32x2 m1095 = m1087 * m1094; SB();
  const f32x2 m1107 = m1099 * m1106; SB();
  const unsigned m1096 = pk(m1095[0], m1095[1]); SB();
  const unsigned m1108 = pk(m1107[0], m1107[1]); SB();
  WAITC(63, 2);
  const f32x2 m1109 = PFMA(LO2(cs1026), n201, LO2(bb1033)); SB();
  const f32x2 m1121 = PFMA(HI2(cs1026), n201, HI2(bb1033)); SB();
  const f32x2 m1111 = PFMA(LO2(a01_3), r201, m1109); SB();
  const f32x2 m1123 = PFMA(HI2(a01_3), r201, m1121); SB();
  const float m1112 = __builtin_amdgcn_exp2f(m1111[0]); SB();
  const float m1113 = __builtin_amdgcn_exp2f(m1111[1]); SB();
  const float m1124 = __builtin_amdgcn_exp2f(m1123[0]); SB();
  const float m1125 = __builtin_amdgcn_exp2f(m1123[1]); SB();
  const f32x4 cs1028 = LDSF4(cbp + 640);
  const f32x4 bb1035 = LDSF4(cbp + 704);
  const f32x2 m1114 = kone + f32x2{m1112, m1113}; SB();
  const f32x2 m1126 = kone + f32x2{m1124, m1125}; SB();
  const f32x2 m1115 = PFMA(m1114, m1114, kone); SB();
  const f32x2 m1127 = PFMA(m1126, m1126, kone); SB();
  const float m1116 = __builtin_amdgcn_rcpf(m1115[0]); SB();
  const float m1117 = __builtin_amdgcn_rcpf(m1115[1]); SB();
  const float m1128 = __builtin_amdgcn_rcpf(m1127[0]); SB();
  const float m1129 = __builtin_amdgcn_rcpf(m1127[1]); SB();
  const f32x2 m1118 = PFMA((f32x2{m1116, m1117}), km2, kln2); SB();
  const f32x2 m1130 = PFMA((f32x2{m1128, m1129}), km2, kln2); SB();
  const f32x2 m1119 = m1111 * m1118; SB();
  const f32x2 m1131 = m1123 * m1130; SB();
  const unsigned m1120 = pk(m1119[0], m1119[1]); SB();
  const unsigned m1132 = pk(m1131[0], m1131[1]); SB();
  st16(v4u{m1096, m1108, m1120, m1132}, ws, vo0 + 1024, hs01);
  WAITC(63, 2);
  const f32x2 m1133 = PFMA(LO2(cs1027), n201, LO2(bb1034)); SB();
  const f32x2 m1145 = PFMA(HI2(cs1027), n201, HI2(bb1034)); SB();
  const f32x2 m1135 = PFMA(LO2(a01_4), r201, m1133); SB();
  const f32x2 m1147 = PFMA(HI2(a01_4), r201, m1145); SB();
  const float m1136 = __builtin_amdgcn_exp2f(m1135[0]); SB();
  const float m1137 = __builtin_amdgcn_exp2f(m1135[1]); SB();
  const float m1148 = __builtin_amdgcn_exp2f(m1147[0]); SB();
  const float m1149 = __builtin_amdgcn_exp2f(m1147[1]); SB();
  const f32x4 cs1029 = LDSF4(cbp + 768);
  const f32x4 bb1036 = LDSF4(cbp + 832);
  const f32x2 m1138 = kone + f32x2{m1136, m1137}; SB();
  const f32x2 m1150 = kone + f32x2{m1148, m1149}; SB();
  const f32x2 m1139 = PFMA(m1138, m1138, kone); SB();
  const f32x2 m1151 = PFMA(m1150, m1150, kone); SB();
  const float m1140 = __builtin_amdgcn_rcpf(m1139[0]); SB();
  const float m1141 = __builtin_amdgcn_rcpf(m1139[1]); SB();
  const float m1152 = __builtin_amdgcn_rcpf(m1151[0]); SB();
  const float m1153 = __builtin_amdgcn_rcpf(m1151[1]); SB();
  const f32x2 m1142 = PFMA((f32x2{m1140, m1141}), km2, kln2); SB();
  const f32x2 m1154 = PFMA((f32x2{m1152, m1153}), km2, kln2); SB();
  const f32x2 m1143 = m1135 * m1142; SB();
  const f32x2 m1155 = m1147 * m1154; SB();
  const unsigned m1144 = pk(m1143[0], m1143[1]); SB();
  const unsigned m1156 = pk(m1155[0], m1155[1]); SB();
  WAITC(63, 2);
  const f32x2 m1157 = PFMA(LO2(cs1028), n201, LO2(bb1035)); SB();
  const f32x2 m1169 = PFMA(HI2(cs1028), n201, HI2(bb1035)); SB();
  const f32x2 m1159 = PFMA(LO2(a01_5), r201, m1157); SB();
  const f32x2 m1171 = PFMA(HI2(a01_5), r201, m1169); SB();
  const float m1160 = __builtin_amdgcn_exp2f(m1159[0]); SB();
  const float m1161 = __builtin_amdgcn_exp2f(m1159[1]); SB();
  const float m1172 = __builtin_amdgcn_exp2f(m1171[0]); SB();
  const float m1173 = __builtin_amdgcn_exp2f(m1171[1]); SB();
  const f32x2 m1162 = kone + f32x2{m1160, m1161}; SB();
  const f32x2 m1174 = kone + f32x2{m1172, m1173}; SB();
  const f32x2 m1163 = PFMA(m1162, m1162, kone); SB();
  const f32x2 m1175 = PFMA(m1174, m1174, kone); SB();
  const float m1164 = __builtin_amdgcn_rcpf(m1163[0]); SB();
  const float m1165 = __builtin_amdgcn_rcpf(m1163[1]); SB();
  const float m1176 = __builtin_amdgcn_rcpf(m1175[0]); SB();
  const float m1177 = __builtin_amdgcn_rcpf(m1175[1]); SB();
  const f32x2 m1166 = PFMA((f32x2{m1164, m1165}), km2, kln2); SB();
  const f32x2 m1178 = PFMA((f32x2{m1176, m1177}), km2, kln2); SB();
  const f32x2 m1167 = m1159 * m1166; SB();
  const f32x2 m1179 = m1171 * m1178; SB();
  const unsigned m1168 = pk(m1167[0], m1167[1]); SB();
  const unsigned m1180 = pk(m1179[0], m1179[1]); SB();
  st16(v4u{m1144, m1156, m1168, m1180}, ws, vo0 + 2048, hs01);
  WAITC(63, 0);
  const f32x2 m1181 = PFMA(LO2(cs1029), n201, LO2(bb1036)); SB();
  const f32x2 m1193 = PFMA(HI2(cs1029), n201, HI2(bb1036)); SB();
  const f32x2 m1183 = PFMA(LO2(a01_6), r201, m1181); SB();
  const f32x2 m1195 = PFMA(HI2(a01_6), r201, m1193); SB();
  const float m1184 = __builtin_amdgcn_exp2f(m1183[0]); SB();
  const float m1185 = __builtin_amdgcn_exp2f(m1183[1]); SB();
  const float m1196 = __builtin_amdgcn_exp2f(m1195[0]); SB();
  const float m1197 = __builtin_amdgcn_exp2f(m1195[1]); SB();
  const f32x2 m1186 = kone + f32x2{m1184, m1185}; SB();
  const f32x2 m1198 = kone + f32x2{m1196, m1197}; SB();
  const f32x2 m1187 = PFMA(m1186, m1186, kone); SB();
  const f32x2 m1199 = PFMA(m1198, m1198, kone); SB();
  const float m1188 = __builtin_amdgcn_rcpf(m1187[0]); SB();
  const float m1189 = __builtin_amdgcn_rcpf(m1187[1]); SB();
  const float m1200 = __builtin_amdgcn_rcpf(m1199[0]); SB();
  const float m1201 = __builtin_amdgcn_rcpf(m1199[1]); SB();
  const f32x2 m1190 = PFMA((f32x2{m1188, m1189}), km2, kln2); SB();
  const f32x2 m1202 = PFMA((f32x2{m1200, m1201}), km2, kln2); SB();
  const f32x2 m1191 = m1183 * m1190; SB();
  const f32x2 m1203 = m1195 * m1202; SB();
  const unsigned m1192 = pk(m1191[0], m1191[1]); SB();
  const unsigned m1204 = pk(m1203[0], m1203[1]); SB();
  st8(v2u{m1192, m1204}, ws, vl8, ht01);
  STAMP(12);
  WAITC(14, 15);
  f32x4 c000_0; MFA0(c000_0, W0, h000_0);
  int so866; SADDS(so866, hb000, ko11);
  v4u h000_11 = ld16(ws, vo0 + 0, so866, SC1);
  int so867; SADD(so867, hb000, 3072);
  f32x4 c000_1; MFA0(c000_1, W1, h000_0);
  f32x4 c000_2; MFA0(c000_2, W2, h000_0);
  f32x4 c000_3; MFA0(c000_3, W3, h000_0);
  f32x4 c000_4; MFA0(c000_4, W4, h000_0);
  WAITC(14, 15);
  const v2u h000_12a = ld8(ws, vha, so867, SC1);
  MFA(c000_0, W5, h000_1);
  MFA(c000_1, W6, h000_1);
  MFA(c000_2, W7, h000_1);
  MFA(c000_3, W8, h000_1);
  const v2u h000_12b = ld8(ws, vhb, so867, SC1);
  int so868; SADD(so868, hb000, 10240);
  MFA(c000_4, W9, h000_1);
  WAITC(15, 15);
  MFA(c000_0, W10, h000_2);
  MFA(c000_1, W11, h000_2);
  MFA(c000_2, W12, h000_2);
  const v2u h000_13a = ld8(ws, vha, so868, SC1);
  MFA(c000_3, W13, h000_2);
  MFA(c000_4, W14, h000_2);
  WAITC(15, 15);
  MFA(c000_0, W15, h000_3);
  MFA(c000_1, W16, h000_3);
  const v2u h000_13b = ld8(ws, vhb, so868, SC1);
  int so869; SADDS(so869, hb001, ko0);
  MFA(c000_2, W17, h000_3);
  MFA(c000_3, W18, h000_3);
  MFA(c000_4, W19, h000_3);
  WAITC(15, 15);
  MFA(c000_0, W20, h000_4);
  v4u h001_0 = ld16(ws, vo0 + 0, so869, SC1);
  int so870; SADDS(so870, hb001, ko1);
  MFA(c000_1, W21, h000_4);
  MFA(c000_2, W22, h000_4);
  MFA(c000_3, W23, h000_4);
  MFA(c000_4, W24, h000_4);
  WAITC(15, 15);
  v4u h001_1 = ld16(ws, vo0 + 0, so870, SC1);
  int so871; SADDS(so871, hb001, ko2);
  MFA(c000_0, W25, h000_5);
  MFA(c000_1, W26, h000_5);
  MFA(c000_2, W27, h000_5);
  MFA(c000_3, W28, h000_5);
  v4u h001_2 = ld16(ws, vo0 + 0, so871, SC1);
  int so872; SADDS(so872, hb001, ko3);
  MFA(c000_4, W29, h000_5);
  WAITC(16, 15);
  MFA(c000_0, W30, h000_6);
  MFA(c000_1, W31, h000_6);
  MFA(c000_2, W32, h000_6);
  v4u h001_3 = ld16(ws, vo0 + 0, so872, SC1);
  int so873; SADDS(so873, hb001, ko4);
  MFA(c000_3, W33, h000_6);
  MFA(c000_4, W34, h000_6);
  WAITC(16, 15);
  MFA(c000_0, W35, h000_7);
  MFA(c000_1, W36, h000_7);
  v4u h001_4 = ld16(ws, vo0 + 0, so873, SC1);
  MFA(c000_2, W37, h000_7);
  MFA(c000_3, W38, h000_7);
  MFA(c000_4, W39, h000_7);
  WAITC(16, 15);
  MFA(c000_0, W40, h000_8);
  DRAINBAR(10);
  MFA(c000_1, W41, h000_8);
  ARRIVE(va1, (gl + 1) * 256);
  MFA(c000_2, W42, h000_8);
  int so874; SADDS(so874, hb001, ko5);
  MFA(c000_3, W43, h000_8);
  MFA(c000_4, W44, h000_8);
  MFA(c000_0, W45, h000_9);
  v4u h001_5 = ld16(ws, vo0 + 0, so874, SC1);
  int so875; SADDS(so875, hb001, ko6);
  MFA(c000_1, W46, h000_9);
  MFA(c000_2, W47, h000_9);
  MFA(c000_3, W48, h000_9);
  MFA(c000_4, W49, h000_9);
  v4u h001_6 = ld16(ws, vo0 + 0, so875, SC1);
  int so876; SADDS(so876, hb001, ko7);
  MFA(c000_0, W50, h000_10);
  MFA(c000_1, W51, h000_10);
  MFA(c000_2, W52, h000_10);
  MFA(c000_3, W53, h000_10);
  v4u h001_7 = ld16(ws, vo0 + 0, so876, SC1);
  int so877; SADDS(so877, hb001, ko8);
  MFA(c000_4, W54, h000_10);
  WAITC(13, 15);
  MFA(c000_0, W55, h000_11);
  MFA(c000_1, W56, h000_11);
  MFA(c000_2, W57, h000_11);
  v4u h001_8 = ld16(ws, vo0 + 0, so877, SC1);
  int so878; SADDS(so878, hb001, ko9);
  MFA(c000_3, W58, h000_11);
  MFA(c000_4, W59, h000_11);
  WAITC(12, 15);
  MFA(c000_0, W60, cat(h000_12a, h000_12b));
  MFA(c000_1, W61, cat(h000_12a, h000_12b));
  v4u h001_9 = ld16(ws, vo0 + 0, so878, SC1);
  int so879; SADDS(so879, hb001, ko10);
  MFA(c000_2, W62, cat(h000_12a, h000_12b));
  MFA(c000_3, W63, cat(h000_12a, h000_12b));
  MFV(c000_4, W64, cat(h000_12a, h000_12b));
  WAITC(11, 15);
  MFV(c000_0, W65, cat(h000_13a, h000_13b));
  v4u h001_10 = ld16(ws, vo0 + 0, so879, SC1);
  int so880; SADDS(so880, hb001, ko11);
  MFV(c000_1, W66, cat(h000_13a, h000_13b));
  MFV(c000_2, W67, cat(h000_13a, h000_13b));
  MFV(c000_3, W68, cat(h000_13a, h000_13b));
  MFV(c000_4, W69, cat(h000_13a, h000_13b));
  WAITC(11, 15);
  v4u h001_11 = ld16(ws, vo0 + 0, so880, SC1);
  int so881; SADD(so881, hb001, 3072);
  f32x4 c001_2; MFA0(c001_2, W2, h001_0);
  f32x4 c001_3; MFA0(c001_3, W3, h001_0);
  f32x4 c001_4; MFA0(c001_4, W4, h001_0);
  WAITC(11, 15);
  MFA(c001_2, W7, h001_1);
  const v2u h001_12a = ld8(ws, vha, so881, SC1);
  MFA(c001_3, W8, h001_1);
  LDSF4(xw0 + 0) = c000_0;
  MFA(c001_4, W9, h001_1);
  WAITC(11, 15);
  MFA(c001_2, W12, h001_2);
  MFA(c001_3, W13, h001_2);
  const v2u h001_12b = ld8(ws, vhb, so881, SC1);
  int so882; SADD(so882, hb001, 10240);
  MFA(c001_4, W14, h001_2);
  WAITC(11, 15);
  LDSF4(xw0 + 1024) = c000_1;
  MFA(c001_2, W17, h001_3);
  MFA(c001_3, W18, h001_3);
  MFA(c001_4, W19, h001_3);
  WAITC(10, 15);
  POLL_LD(pl1205, vp1, (gl2 + 1) * 256);
  MFA(c001_2, W22, h001_4);
  LDSF4(xw2 + 2048) = c000_2;
  MFA(c001_3, W23, h001_4);
  MFA(c001_4, W24, h001_4);
  WAITC(9, 15);
  MFA(c001_2, W27, h001_5);
  const v2u h001_13a = ld8(ws, vha, so882, SC1);
  MFA(c001_3, W28, h001_5);
  LDSF4(xw2 + 3072) = c000_3;
  MFA(c001_4, W29, h001_5);
  WAITC(9, 15);
  MFA(c001_2, W32, h001_6);
  MFA(c001_3, W33, h001_6);
  const v2u h001_13b = ld8(ws, vhb, so882, SC1);
  MFA(c001_4, W34, h001_6);
  WAITC(9, 15);
  LDSF4(xw2 + 4096) = c000_4;
  MFA(c001_2, W37, h001_7);
  MFA(c001_3, W38, h001_7);
  MFA(c001_4, W39, h001_7);
  WAITC(8, 15);
  MFA(c001_2, W42, h001_8);
  MFA(c001_3, W43, h001_8);
  WAITC(63, 0);
  BARRIER();
  const f32x4 xa1239 = LDSF4(xr0 + 0);
  MFA(c001_4, W44, h001_8);
  WAITC(7, 15);
  const f32x4 xb1240 = LDSF4(xr1 + 0);
  const f32x4 xa1241 = LDSF4(xr0 + 1024);
  MFA(c001_2, W47, h001_9);
  const f32x4 xb1242 = LDSF4(xr1 + 1024);
  const f32x4 xa1243 = LDSF4(xr0 + 2048);
  MFA(c001_3, W48, h001_9);
  const f32x4 xb1244 = LDSF4(xr1 + 2048);
  MFA(c001_4, W49, h001_9);
  WAITC(6, 15);
  MFA(c001_2, W52, h001_10);
  MFA(c001_3, W53, h001_10);
  WAITC(63, 4);
  float z1245; OP2("v_add_f32", z1245, xa1239[0], xb1240[0]);
  float z1246; OP2("v_add_f32", z1246, xa1239[1], xb1240[1]);
  MFA(c001_4, W54, h001_10);
  WAITC(5, 15);
  float z1247; OP2("v_add_f32", z1247, xa1239[2], xb1240[2]);
  float z1248; OP2("v_add_f32", z1248, xa1239[3], xb1240[3]);
  MFA(c001_2, W57, h001_11);
  WAITC(63, 2);
  float z1249; OP2("v_add_f32", z1249, xa1241[0], xb1242[0]);
  float z1250; OP2("v_add_f32", z1250, xa1241[1], xb1242[1]);
  MFA(c001_3, W58, h001_11);
  float z1251; OP2("v_add_f32", z1251, xa1241[2], xb1242[2]);
  float z1252; OP2("v_add_f32", z1252, xa1241[3], xb1242[3]);
  MFA(c001_4, W59, h001_11);
  WAITC(3, 0);
  float z1253; OP2("v_add_f32", z1253, xa1243[0], xb1244[0]);
  float z1254; OP2("v_add_f32", z1254, xa1243[1], xb1244[1]);
  MFA(c001_2, W62, cat(h001_12a, h001_12b));
  float z1255; OP2("v_add_f32", z1255, xa1243[2], xb1244[2]);
  float z1256; OP2("v_add_f32", z1256, xa1243[3], xb1244[3]);
  MFA(c001_3, W63, cat(h001_12a, h001_12b));
  MFV(c001_4, W64, cat(h001_12a, h001_12b));
  WAITC(0, 15);
  POLL_CK(pl1205, vp1, (gl2 + 1) * 256, 16);
  MFV(c001_2, W67, cat(h001_13a, h001_13b));
  int so1206; SADDS(so1206, hb010, ko0);
  v4u h010_0 = ld16(ws, vo0 + 0, so1206, SC1);
  int so1207; SADDS(so1207, hb010, ko1);
  MFV(c001_3, W68, cat(h001_13a, h001_13b));
  MFV(c001_4, W69, cat(h001_13a, h001_13b));
  f32x4 c001_0; MFA0(c001_0, W0, h001_0);
  f32x4 c001_1; MFA0(c001_1, W1, h001_0);
  v4u h010_1 = ld16(ws, vo0 + 0, so1207, SC1);
  int so1208; SADDS(so1208, hb010, ko2);
  MFA(c001_0, W5, h001_1);
  MFA(c001_1, W6, h001_1);
  MFA(c001_0, W10, h001_2);
  LDSF4(xr1) = c001_2;
  MFA(c001_1, W11, h001_2);
  v4u h010_2 = ld16(ws, vo0 + 0, so1208, SC1);
  int so1209; SADDS(so1209, hb010, ko3);
  MFA(c001_0, W15, h001_3);
  MFA(c001_1, W16, h001_3);
  MFA(c001_0, W20, h001_4);
  LDSF4(xr0) = c001_3;
  MFA(c001_1, W21, h001_4);
  v4u h010_3 = ld16(ws, vo0 + 0, so1209, SC1);
  int so1210; SADDS(so1210, hb010, ko4);
  MFA(c001_0, W25, h001_5);
  MFA(c001_1, W26, h001_5);
  MFA(c001_0, W30, h001_6);
  LDSF4(xr0 + 1024) = c001_4;
  MFA(c001_1, W31, h001_6);
  float z1260; OP2("v_add_f32", z1260, z1253, c001_2[0]);
  float z1261; OP2("v_add_f32", z1261, z1254, c001_2[1]);
  MFA(c001_0, W35, h001_7);
  float z1262; OP2("v_add_f32", z1262, z1255, c001_2[2]);
  float z1263; OP2("v_add_f32", z1263, z1256, c001_2[3]);
  MFA(c001_1, W36, h001_7);
  v4u h010_4 = ld16(ws, vo0 + 0, so1210, SC1);
  int so1211; SADDS(so1211, hb010, ko5);
  MFA(c001_0, W40, h001_8);
  WAITC(63, 0);

  BARRIER();
  const f32x4 ya1264 = LDSF4(yq0);
  MFA(c001_1, W41, h001_8);
  const f32x4 ya1265 = LDSF4(yq1);
  const f32x4 ya1266 = LDSF4(yr2a + 1024);
  MFA(c001_0, W45, h001_9);
  MFA(c001_1, W46, h001_9);
  v4u h010_5 = ld16(ws, vo0 + 0, so1211, SC1);
  int so1212; SADDS(so1212, hb010, ko6);
  MFA(c001_0, W50, h001_10);
  MFA(c001_1, W51, h001_10);
  MFA(c001_0, W55, h001_11);
  MFA(c001_1, W56, h001_11);
  v4u h010_6 = ld16(ws, vo0 + 0, so1212, SC1);
  MFA(c001_0, W60, cat(h001_12a, h001_12b));
  MFA(c001_1, W61, cat(h001_12a, h001_12b));
  MFV(c001_0, W65, cat(h001_13a, h001_13b));
  MFV(c001_1, W66, cat(h001_13a, h001_13b));
  WAITC(63, 0);
  STAMP(13);
  NOP8(); TIE(c001_0); TIE(c001_1);
  f32x4 r00_0 = (f32x4{z1245, z1246, z1247, z1248} + c001_0) + ya1264;
  f32x4 r00_1 = (f32x4{z1249, z1250, z1251, z1252} + c001_1) + ya1265;
  f32x4 r00_2 = (f32x4{z1260, z1261, z1262, z1263}) + ya1266;
  int so1213; SADDS(so1213, hb010, ko7);
  v4u h010_7 = ld16(ws, vo0 + 0, so1213, SC1);
  int so1214; SADDS(so1214, hb010, ko8);
  v4u h010_8 = ld16(ws, vo0 + 0, so1214, SC1);
  EPI2A(0);
  int so1215; SADDS(so1215, hb010, ko9);
  v4u h010_9 = ld16(ws, vo0 + 0, so1215, SC1);
  int so1216; SADDS(so1216, hb010, ko10);
  v4u h010_10 = ld16(ws, vo0 + 0, so1216, SC1);
  int so1217; SADDS(so1217, hb010, ko11);
  v4u h010_11 = ld16(ws, vo0 + 0, so1217, SC1);
  STAMP(14);
  WAITC(12, 15);
  f32x4 c010_0; MFA0(c010_0, W0, h010_0);
  int so1218; SADD(so1218, hb010, 3072);
  const v2u h010_12a = ld8(ws, vha, so1218, SC1);
  f32x4 c010_1; MFA0(c010_1, W1, h010_0);
  f32x4 c010_2; MFA0(c010_2, W2, h010_0);
  f32x4 c010_3; MFA0(c010_3, W3, h010_0);
  const v2u h010_12b = ld8(ws, vhb, so1218, SC1);
  int so1219; SADD(so1219, hb010, 10240);
  f32x4 c010_4; MFA0(c010_4, W4, h010_0);
  WAITC(13, 15);
  MFA(c010_0, W5, h010_1);
  MFA(c010_1, W6, h010_1);
  const v2u h010_13a = ld8(ws, vha, so1219, SC1);
  MFA(c010_2, W7, h010_1);
  MFA(c010_3, W8, h010_1);
  MFA(c010_4, W9, h010_1);
  WAITC(13, 15);
  const v2u h010_13b = ld8(ws, vhb, so1219, SC1);
  int so1220; SADDS(so1220, hb011, ko0);
  MFA(c010_0, W10, h010_2);
  MFA(c010_1, W11, h010_2);
  MFA(c010_2, W12, h010_2);
  v4u h011_0 = ld16(ws, vo0 + 0, so1220, SC1);
  int so1221; SADDS(so1221, hb011, ko1);
  MFA(c010_3, W13, h010_2);
  MFA(c010_4, W14, h010_2);
  WAITC(14, 15);
  MFA(c010_0, W15, h010_3);
  v4u h011_1 = ld16(ws, vo0 + 0, so1221, SC1);
  int so1222; SADDS(so1222, hb011, ko2);
  MFA(c010_1, W16, h010_3);
  MFA(c010_2, W17, h010_3);
  MFA(c010_3, W18, h010_3);
  v4u h011_2 = ld16(ws, vo0 + 0, so1222, SC1);
  int so1223; SADDS(so1223, hb011, ko3);
  MFA(c010_4, W19, h010_3);
  WAITC(15, 15);
  MFA(c010_0, W20, h010_4);

  MFA(c010_1, W21, h010_4);
  v4u h011_3 = ld16(ws, vo0 + 0, so1223, SC1);
  int so1224; SADDS(so1224, hb011, ko4);
  MFA(c010_2, W22, h010_4);
  MFA(c010_3, W23, h010_4);
  MFA(c010_4, W24, h010_4);
  WAITC(15, 15);
  v4u h011_4 = ld16(ws, vo0 + 0, so1224, SC1);
  int so1225; SADDS(so1225, hb011, ko5);
  MFA(c010_0, W25, h010_5);
  MFA(c010_1, W26, h010_5);
  MFA(c010_2, W27, h010_5);
  v4u h011_5 = ld16(ws, vo0 + 0, so1225, SC1);
  int so1226; SADDS(so1226, hb011, ko6);
  MFA(c010_3, W28, h010_5);
  MFA(c010_4, W29, h010_5);
  WAITC(16, 15);
  MFA(c010_0, W30, h010_6);
  v4u g0_0 = ld16(ws, vg + 0, gb0, SC1);
  MFA(c010_1, W31, h010_6);
  MFA(c010_2, W32, h010_6);
  MFA(c010_3, W33, h010_6);
  v4u g0_1 = ld16(ws, vg + 256, gb0, SC1);
  MFA(c010_4, W34, h010_6);
  WAITC(17, 15);
  MFA(c010_0, W35, h010_7);
  MFA(c010_1, W36, h010_7);
  v4u g0_2 = ld16(ws, vg + 512, gb0, SC1);
  MFA(c010_2, W37, h010_7);
  MFA(c010_3, W38, h010_7);
  MFA(c010_4, W39, h010_7);
  WAITC(17, 15);
  v4u g0_3 = ld16(ws, vg + 768, gb0, SC1);
  MFA(c010_0, W40, h010_8);
  BARRIER();
  MFA(c010_1, W41, h010_8);
  MFA(c010_2, W42, h010_8);
  v4u h011_6 = ld16(ws, vo0 + 0, so1226, SC1);
  int so1227; SADDS(so1227, hb011, ko7);
  MFA(c010_3, W43, h010_8);
  MFA(c010_4, W44, h010_8);
  WAITC(17, 15);
  MFA(c010_0, W45, h010_9);
  v4u h011_7 = ld16(ws, vo0 + 0, so1227, SC1);
  int so1228; SADDS(so1228, hb011, ko8);
  MFA(c010_1, W46, h010_9);
  MFA(c010_2, W47, h010_9);
  MFA(c010_3, W48, h010_9);
  v4u h011_8 = ld16(ws, vo0 + 0, so1228, SC1);
  int so1229; SADDS(so1229, hb011, ko9);
  MFA(c010_4, W49, h010_9);
  WAITC(18, 15);
  MFA(c010_0, W50, h010_10);
  MFA(c010_1, W51, h010_10);
  v4u h011_9 = ld16(ws, vo0 + 0, so1229, SC1);
  int so1230; SADDS(so1230, hb011, ko10);
  MFA(c010_2, W52, h010_10);
  MFA(c010_3, W53, h010_10);
  MFA(c010_4, W54, h010_10);
  WAITC(18, 15);
  v4u h011_10 = ld16(ws, vo0 + 0, so1230, SC1);
  int so1231; SADDS(so1231, hb011, ko11);
  MFA(c010_0, W55, h010_11);
  MFA(c010_1, W56, h010_11);
  MFA(c010_2, W57, h010_11);
  v4u h011_11 = ld16(ws, vo0 + 0, so1231, SC1);
  int so1232; SADD(so1232, hb011, 3072);
  MFA(c010_3, W58, h010_11);
  MFA(c010_4, W59, h010_11);
  WAITC(18, 15);
  MFA(c010_0, W60, cat(h010_12a, h010_12b));
  const v2u h011_12a = ld8(ws, vha, so1232, SC1);
  MFA(c010_1, W61, cat(h010_12a, h010_12b));
  MFA(c010_2, W62, cat(h010_12a, h010_12b));
  MFA(c010_3, W63, cat(h010_12a, h010_12b));
  const v2u h011_12b = ld8(ws, vhb, so1232, SC1);
  int so1233; SADD(so1233, hb011, 10240);
  MFV(c010_4, W64, cat(h010_12a, h010_12b));
  WAITC(18, 15);
  MFV(c010_0, W65, cat(h010_13a, h010_13b));
  MFV(c010_1, W66, cat(h010_13a, h010_13b));
  const v2u h011_13a = ld8(ws, vha, so1233, SC1);
  MFV(c010_2, W67, cat(h010_13a, h010_13b));
  MFV(c010_3, W68, cat(h010_13a, h010_13b));
  MFV(c010_4, W69, cat(h010_13a, h010_13b));
  WAITC(9, 15);
  STAMP(15);
  GATHER_CK(0);
  EPI2B(0);
  STAMP(16);
  f32x4 c011_2; MFA0(c011_2, W2, h011_0);
  LDSF4(xw0 + 0) = c010_0;
  f32x4 c011_3; MFA0(c011_3, W3, h011_0);
  const v2u h011_13b = ld8(ws, vhb, so1233, SC1);
  f32x4 c011_4; MFA0(c011_4, W4, h011_0);
  MFA(c011_2, W7, h011_1);
  MFA(c011_3, W8, h011_1);
  LDSF4(xw0 + 1024) = c010_1;
  MFA(c011_4, W9, h011_1);
  const v2u e11_0 = ld8(ws, vxe + 0, ex1, SC1);
  MFA(c011_2, W12, h011_2);
  MFA(c011_3, W13, h011_2);
  MFA(c011_4, W14, h011_2);
  LDSF4(xw2 + 2048) = c010_2;
  MFA(c011_2, W17, h011_3);
  const v2u ey1_0 = ld8(ws, vxe + 0, ey1, SC1);
  MFA(c011_3, W18, h011_3);
  MFA(c011_4, W19, h011_3);
  MFA(c011_2, W22, h011_4);
  LDSF4(xw2 + 3072) = c010_3;
  MFA(c011_3, W23, h011_4);
  const v2u e11_1 = ld8(ws, vxe + 512, ex1, SC1);
  MFA(c011_4, W24, h011_4);
  MFA(c011_2, W27, h011_5);
  MFA(c011_3, W28, h011_5);
  LDSF4(xw2 + 4096) = c010_4;
  MFA(c011_4, W29, h011_5);
  WAITC(15, 15);
  const v2u ey1_1 = ld8(ws, vxe + 512, ey1, SC1);
  MFA(c011_2, W32, h011_6);
  MFA(c011_3, W33, h011_6);
  MFA(c011_4, W34, h011_6);
  WAITC(15, 15);
  MFA(c011_2, W37, h011_7);
  WAITC(63, 0);
  BARRIER();
  const f32x4 xa1272 = LDSF4(xr0 + 0);
  MFA(c011_3, W38, h011_7);
  const f32x4 xb1273 = LDSF4(xr1 + 0);
  const f32x4 xa1274 = LDSF4(xr0 + 1024);
  MFA(c011_4, W39, h011_7);
  WAITC(14, 15);
  const f32x4 xb1275 = LDSF4(xr1 + 1024);
  const f32x4 xa1276 = LDSF4(xr0 + 2048);
  MFA(c011_2, W42, h011_8);
  const f32x4 xb1277 = LDSF4(xr1 + 2048);
  const v2u e11_2 = ld8(ws, vxe + 0, ex31, SC1);
  MFA(c011_3, W43, h011_8);
  MFA(c011_4, W44, h011_8);
  WAITC(14, 15);
  MFA(c011_2, W47, h011_9);
  WAITC(63, 4);
  float z1278; OP2("v_add_f32", z1278, xa1272[0], xb1273[0]);
  float z1279; OP2("v_add_f32", z1279, xa1272[1], xb1273[1]);
  MFA(c011_3, W48, h011_9);
  float z1280; OP2("v_add_f32", z1280, xa1272[2], xb1273[2]);
  float z1281; OP2("v_add_f32", z1281, xa1272[3], xb1273[3]);
  MFA(c011_4, W49, h011_9);
  WAITC(13, 2);
  float z1282; OP2("v_add_f32", z1282, xa1274[0], xb1275[0]);
  float z1283; OP2("v_add_f32", z1283, xa1274[1], xb1275[1]);
  MFA(c011_2, W52, h011_10);
  float z1284; OP2("v_add_f32", z1284, xa1274[2], xb1275[2]);
  float z1285; OP2("v_add_f32", z1285, xa1274[3], xb1275[3]);
  MFA(c011_3, W53, h011_10);
  WAITC(63, 0);
  float z1286; OP2("v_add_f32", z1286, xa1276[0], xb1277[0]);
  float z1287; OP2("v_add_f32", z1287, xa1276[1], xb1277[1]);
  MFA(c011_4, W54, h011_10);
  WAITC(12, 15);
  float z1288; OP2("v_add_f32", z1288, xa1276[2], xb1277[2]);
  float z1289; OP2("v_add_f32", z1289, xa1276[3], xb1277[3]);
  MFA(c011_2, W57, h011_11);
  const v2u ey1_2 = ld8(ws, vxe + 0, ey31, SC1);
  MFA(c011_3, W58, h011_11);
  MFA(c011_4, W59, h011_11);
  WAITC(11, 15);
  MFA(c011_2, W62, cat(h011_12a, h011_12b));
  MFA(c011_3, W63, cat(h011_12a, h011_12b));
  MFV(c011_4, W64, cat(h011_12a, h011_12b));
  WAITC(6, 15);
  MFV(c011_2, W67, cat(h011_13a, h011_13b));
  MFV(c011_3, W68, cat(h011_13a, h011_13b));
  MFV(c011_4, W69, cat(h011_13a, h011_13b));
  f32x4 c011_0; MFA0(c011_0, W0, h011_0);
  f32x4 c011_1; MFA0(c011_1, W1, h011_0);
  MFA(c011_0, W5, h011_1);
  MFA(c011_1, W6, h011_1);
  MFA(c011_0, W10, h011_2);
  LDSF4(xr1) = c011_2;
  MFA(c011_1, W11, h011_2);
  MFA(c011_0, W15, h011_3);
  MFA(c011_1, W16, h011_3);
  MFA(c011_0, W20, h011_4);
  LDSF4(xr0) = c011_3;
  MFA(c011_1, W21, h011_4);
  MFA(c011_0, W25, h011_5);
  MFA(c011_1, W26, h011_5);
  MFA(c011_0, W30, h011_6);
  LDSF4(xr0 + 1024) = c011_4;
  MFA(c011_1, W31, h011_6);
  float z1293; OP2("v_add_f32", z1293, z1286, c011_2[0]);
  float z1294; OP2("v_add_f32", z1294, z1287, c011_2[1]);
  MFA(c011_0, W35, h011_7);
  float z1295; OP2("v_add_f32", z1295, z1288, c011_2[2]);
  float z1296; OP2("v_add_f32", z1296, z1289, c011_2[3]);
  MFA(c011_1, W36, h011_7);
  MFA(c011_0, W40, h011_8);
  WAITC(63, 0);

  DRAINBAR(7);
  MFA(c011_1, W41, h011_8);
  ARRIVE(va1, (gl2 + 2) * 256);
  MFA(c011_0, W45, h011_9);
  const f32x4 ya1297 = LDSF4(yq0);
  const f32x4 ya1298 = LDSF4(yq1);
  MFA(c011_1, W46, h011_9);
  const f32x4 ya1299 = LDSF4(yr2a + 1024);
  MFA(c011_0, W50, h011_10);
  MFA(c011_1, W51, h011_10);
  MFA(c011_0, W55, h011_11);
  MFA(c011_1, W56, h011_11);
  MFA(c011_0, W60, cat(h011_12a, h011_12b));
  MFA(c011_1, W61, cat(h011_12a, h011_12b));
  MFV(c011_0, W65, cat(h011_13a, h011_13b));
  MFV(c011_1, W66, cat(h011_13a, h011_13b));
  WAITC(2, 0);
  NOP8(); TIE(c011_0); TIE(c011_1);
  f32x4 r01_0 = (f32x4{z1278, z1279, z1280, z1281} + c011_0) + ya1297;
  f32x4 r01_1 = (f32x4{z1282, z1283, z1284, z1285} + c011_1) + ya1298;
  f32x4 r01_2 = (f32x4{z1293, z1294, z1295, z1296}) + ya1299;
  EPI2A(1);
  POLL_LD(pl1300, vp1, (gl + 2) * 256);
  STAMP(17);
  WAITC(0, 15);
  POLL_CK(pl1300, vp1, (gl + 2) * 256, 16);
  CB_STORE();
  STAMP(18);
  v4u g1_0 = ld16(ws, vg + 0, gb1, SC1);
  v4u g1_1 = ld16(ws, vg + 256, gb1, SC1);
  v4u g1_2 = ld16(ws, vg + 512, gb1, SC1);
  v4u g1_3 = ld16(ws, vg + 768, gb1, SC1);
  const v2u sp0_0 = ld8(ws, vsp + 0, spb0, SC1);
  const v2u sp0_1 = ld8(ws, vsp + 128, spb0, SC1);
  const v2u sp0_2 = ld8(ws, vsp + 256, spb0, SC1);
  const v2u sp0_3 = ld8(ws, vsp + 384, spb0, SC1);
  int so1301; SADDS(so1301, xb10, xo0);
  v4u x10_0 = ld16(ws, vo0 + 0, so1301, SC1);
  v4u x10_1 = ld16(ws, vo0 + 1024, so1301, SC1);
  v4u x10_2 = ld16(ws, vo0 + 2048, so1301, SC1);
  v4u x10_3 = ld16(ws, vo0 + 3072, so1301, SC1);
  int so1302; SADDS(so1302, xb10, xo1);
  v4u x10_4 = ld16(ws, vo0 + 0, so1302, SC1);
  v4u x10_5 = ld16(ws, vo0 + 1024, so1302, SC1);
  v4u x10_6 = ld16(ws, vo0 + 2048, so1302, SC1);
  v4u x10_7 = ld16(ws, vo0 + 3072, so1302, SC1);
  WAITC(12, 15);
  GATHER_CK(1);
  const v4u w1312 = LDS16(wb0 + 0);
  const v4u w1313 = LDS16(wb0 + 1024);
  const v4u w1314 = LDS16(wb0 + 2048);
  const v4u w1315 = LDS16(wb0 + 3072);
  const v4u w1316 = LDS16(wb0 + 4096);
  const v4u w1317 = LDS16(wb0 + 5120);
  const v4u w1318 = LDS16(wb0 + 6144);
  const v4u w1319 = LDS16(wb0 + 7168);
  EPI2B(1);
  STAMP(19);
  WAITC(11, 15);
  float rs10, nm10; lnstat(xsum_g((__uint_as_float(sp0_0.x) + __uint_as_float(sp0_1.x)) + (__uint_as_float(sp0_2.x) + __uint_as_float(sp0_3.x))), xsum_g((__uint_as_float(sp0_0.y) + __uint_as_float(sp0_1.y)) + (__uint_as_float(sp0_2.y) + __uint_as_float(sp0_3.y))), rs10, nm10); TIE(rs10); TIE(nm10);
  WAITC(10, 7);
  f32x4 a10_0; MFV0(a10_0, w1312, x10_0);
  int so1303; SADDS(so1303, xb10, xo2);
  const v4u w1320 = LDS16(wb0 + 8192);
  v4u x10_8 = ld16(ws, vo0 + 0, so1303, SC1);
  WAITC(63, 7);
  f32x4 a10_1; MFV0(a10_1, w1313, x10_0);
  WAITC(63, 6);
  const v4u w1321 = LDS16(wb0 + 9216);
  f32x4 a10_2; MFV0(a10_2, w1314, x10_0);
  WAITC(63, 6);
  const v4u w1322 = LDS16(wb0 + 10240);
  f32x4 a10_3; MFV0(a10_3, w1315, x10_0);
  WAITC(63, 6);
  const v4u w1323 = LDS16(wb0 + 11264);
  f32x4 a10_4; MFV0(a10_4, w1316, x10_0);
  WAITC(63, 6);
  const v4u w1324 = LDS16(wb0 + 12288);
  v4u x10_9 = ld16(ws, vo0 + 1024, so1303, SC1);
  f32x4 a10_5; MFV0(a10_5, w1317, x10_0);
  WAITC(63, 6);
  const v4u w1325 = LDS16(wb0 + 13312);
  f32x4 a10_6; MFV0(a10_6, w1318, x10_0);
  WAITC(11, 6);
  const v4u w1326 = LDS16(wb0 + 14336);
  MFV(a10_0, w1319, x10_1);
  WAITC(63, 6);
  const v4u w1327 = LDS16(wb0 + 15360);
  MFV(a10_1, w1320, x10_1);
  WAITC(63, 6);
  const v4u w1328 = LDS16(wb0 + 16384);
  v4u x10_10 = ld16(ws, vo0 + 2048, so1303, SC1);
  MFV(a10_2, w1321, x10_1);
  WAITC(63, 6);
  const v4u w1329 = LDS16(wb0 + 17408);
  MFV(a10_3, w1322, x10_1);
  WAITC(63, 6);
  const v4u w1330 = LDS16(wb0 + 18432);
  MFV(a10_4, w1323, x10_1);
  WAITC(63, 6);
  const v4u w1331 = LDS16(wb0 + 19456);
  MFV(a10_5, w1324, x10_1);
  WAITC(11, 5);
  const v4u w1332 = LDS16(wb0 + 20480);
  v4u x10_11 = ld16(ws, vo0 + 3072, so1303, SC1);
  MFV(a10_6, w1325, x10_1);
  int so1304; SADDS(so1304, xb10, xo3);
  const v4u w1333 = LDS16(wb0 + 21504);
  MFV(a10_0, w1326, x10_2);
  WAITC(63, 6);
  const v4u w1334 = LDS16(wb0 + 22528);
  MFV(a10_1, w1327, x10_2);
  WAITC(63, 6);
  const v4u w1335 = LDS16(wb0 + 23552);
  MFV(a10_2, w1328, x10_2);
  WAITC(63, 6);
  const v4u w1336 = LDS16(wb0 + 24576);
  v4u x10_12 = ld16(ws, vo0 + 0, so1304, SC1);
  MFV(a10_3, w1329, x10_2);
  WAITC(63, 6);
  const v4u w1337 = LDS16(wb0 + 25600);
  MFV(a10_4, w1330, x10_2);
  WAITC(63, 6);
  const v4u w1338 = LDS16(wb0 + 26624);
  MFV(a10_5, w1331, x10_2);
  WAITC(63, 6);
  const v4u w1339 = LDS16(wb0 + 27648);
  MFV(a10_6, w1332, x10_2);
  WAITC(12, 6);
  const v4u w1340 = LDS16(wb0 + 28672);
  v4u x10_13 = ld16(ws, vo0 + 1024, so1304, SC1);
  MFV(a10_0, w1333, x10_3);
  WAITC(63, 6);
  const v4u w1341 = LDS16(wb0 + 29696);
  MFV(a10_1, w1334, x10_3);
  WAITC(63, 6);
  const v4u w1342 = LDS16(wb0 + 30720);
  MFV(a10_2, w1335, x10_3);
  WAITC(63, 6);
  const v4u w1343 = LDS16(wb0 + 31744);
  MFV(a10_3, w1336, x10_3);
  WAITC(63, 6);
  const v4u w1344 = LDS16(wb0 + 32768);
  v4u x10_14 = ld16(ws, vo0 + 2048, so1304, SC1);
  MFV(a10_4, w1337, x10_3);
  WAITC(63, 6);
  const v4u w1345 = LDS16(wb0 + 33792);
  MFV(a10_5, w1338, x10_3);
  WAITC(63, 6);
  const v4u w1346 = LDS16(wb0 + 34816);
  MFV(a10_6, w1339, x10_3);
  WAITC(13, 6);
  const v4u w1347 = LDS16(wb0 + 35840);
  MFV(a10_0, w1340, x10_4);
  WAITC(63, 6);
  const v4u w1348 = LDS16(wb0 + 36864);
  v4u x10_15 = ld16(ws, vo0 + 3072, so1304, SC1);
  MFV(a10_1, w1341, x10_4);
  WAITC(63, 6);
  const v4u w1349 = LDS16(wb0 + 37888);
  MFV(a10_2, w1342, x10_4);
  WAITC(63, 6);
  const v4u w1350 = LDS16(wb0 + 38912);
  MFV(a10_3, w1343, x10_4);
  WAITC(63, 6);
  const v4u w1351 = LDS16(wb0 + 39936);
  MFV(a10_4, w1344, x10_4);
  WAITC(63, 6);
  const v4u w1352 = LDS16(wb0 + 40960);
  MFV(a10_5, w1345, x10_4);
  WAITC(63, 6);
  const v4u w1353 = LDS16(wb0 + 41984);
  MFV(a10_6, w1346, x10_4);
  WAITC(13, 6);
  const v4u w1354 = LDS16(wb0 + 43008);
  MFV(a10_0, w1347, x10_5);
  WAITC(63, 6);
  const v4u w1355 = LDS16(wb0 + 44032);
  WAITC(63, 7);
  DRAINBAR(8);
  MFV(a10_1, w1348, x10_5);
  WAITC(63, 5);
  const v4u w1356 = LDS16(wb0 + 45056);
  ARRIVE(va1, (gl2 + 3) * 256);
  MFV(a10_2, w1349, x10_5);
  int so1305; SADDS(so1305, xb10, xo4);
  const v4u w1357 = LDS16(wb0 + 46080);
  MFV(a10_3, w1350, x10_5);
  WAITC(63, 6);
  const v4u w1358 = LDS16(wb0 + 47104);
  MFV(a10_4, w1351, x10_5);
  WAITC(63, 6);
  const v4u w1359 = LDS16(wb0 + 48128);
  MFV(a10_5, w1352, x10_5);
  WAITC(63, 6);
  const v4u w1360 = LDS16(wb0 + 49152);
  v4u x10_16 = ld16(ws, vo0 + 0, so1305, SC1);
  MFV(a10_6, w1353, x10_5);
  WAITC(63, 6);
  const v4u w1361 = LDS16(wb0 + 50176);
  MFV(a10_0, w1354, x10_6);
  WAITC(63, 6);
  const v4u w1362 = LDS16(wb0 + 51200);
  MFV(a10_1, w1355, x10_6);
  WAITC(63, 6);
  const v4u w1363 = LDS16(wb0 + 52224);
  MFV(a10_2, w1356, x10_6);
  WAITC(63, 6);
  const v4u w1364 = LDS16(wb0 + 53248);
  v4u x10_17 = ld16(ws, vo0 + 1024, so1305, SC1);
  MFV(a10_3, w1357, x10_6);
  WAITC(63, 6);
  const v4u w1365 = LDS16(wb0 + 54272);
  MFV(a10_4, w1358, x10_6);
  WAITC(63, 6);
  const v4u w1366 = LDS16(wb0 + 55296);
  MFV(a10_5, w1359, x10_6);
  WAITC(63, 6);
  const v4u w1367 = LDS16(wb0 + 56320);
  MFV(a10_6, w1360, x10_6);
  WAITC(63, 6);
  const v4u w1368 = LDS16(wb0 + 57344);
  v4u x10_18 = ld16(ws, vo0 + 2048, so1305, SC1);
  MFV(a10_0, w1361, x10_7);
  WAITC(63, 6);
  const v4u w1369 = LDS16(wb0 + 58368);
  MFV(a10_1, w1362, x10_7);
  WAITC(63, 6);
  const v4u w1370 = LDS16(wb0 + 59392);
  MFV(a10_2, w1363, x10_7);
  WAITC(63, 6);
  const v4u w1371 = LDS16(wb0 + 60416);
  MFV(a10_3, w1364, x10_7);
  WAITC(63, 6);
  const v4u w1372 = LDS16(wb0 + 61440);
  v4u x10_19 = ld16(ws, vo0 + 3072, so1305, SC1);
  MFV(a10_4, w1365, x10_7);
  WAITC(63, 6);
  const v4u w1373 = LDS16(wb0 + 62464);
  MFV(a10_5, w1366, x10_7);
  WAITC(63, 6);
  const v4u w1374 = LDS16(wb0 + 63488);
  MFV(a10_6, w1367, x10_7);
  WAITC(12, 6);
  const v4u w1375 = LDS16(wb1 + 0);
  MFV(a10_0, w1368, x10_8);
  WAITC(63, 6);
  const v4u w1376 = LDS16(wb1 + 1024);
  MFV(a10_1, w1369, x10_8);
  WAITC(63, 6);
  const v4u w1377 = LDS16(wb1 + 2048);
  MFV(a10_2, w1370, x10_8);
  WAITC(63, 6);
  const v4u w1378 = LDS16(wb1 + 3072);
  MFV(a10_3, w1371, x10_8);
  WAITC(63, 6);
  const v4u w1379 = LDS16(wb1 + 4096);
  MFV(a10_4, w1372, x10_8);
  WAITC(63, 6);
  const v4u w1380 = LDS16(wb1 + 5120);
  MFV(a10_5, w1373, x10_8);
  WAITC(63, 6);
  const v4u w1381 = LDS16(wb1 + 6144);
  MFV(a10_6, w1374, x10_8);
  WAITC(11, 6);
  const v4u w1382 = LDS16(wb1 + 7168);
  MFV(a10_0, w1375, x10_9);
  WAITC(63, 6);
  const v4u w1383 = LDS16(wb1 + 8192);
  MFV(a10_1, w1376, x10_9);
  WAITC(63, 6);
  const v4u w1384 = LDS16(wb1 + 9216);
  MFV(a10_2, w1377, x10_9);
  WAITC(63, 6);
  const v4u w1385 = LDS16(wb1 + 10240);
  MFV(a10_3, w1378, x10_9);
  WAITC(63, 6);
  const v4u w1386 = LDS16(wb1 + 11264);
  MFV(a10_4, w1379, x10_9);
  WAITC(63, 6);
  const v4u w1387 = LDS16(wb1 + 12288);
  MFV(a10_5, w1380, x10_9);
  WAITC(63, 6);
  const v4u w1388 = LDS16(wb1 + 13312);
  MFV(a10_6, w1381, x10_9);
  WAITC(10, 6);
  const v4u w1389 = LDS16(wb1 + 14336);
  MFV(a10_0, w1382, x10_10);
  WAITC(63, 6);
  const v4u w1390 = LDS16(wb1 + 15360);
  MFV(a10_1, w1383, x10_10);
  WAITC(63, 6);
  const v4u w1391 = LDS16(wb1 + 16384);
  MFV(a10_2, w1384, x10_10);
  WAITC(63, 6);
  const v4u w1392 = LDS16(wb1 + 17408);
  MFV(a10_3, w1385, x10_10);
  WAITC(63, 6);
  const v4u w1393 = LDS16(wb1 + 18432);
  MFV(a10_4, w1386, x10_10);
  WAITC(63, 6);
  const v4u w1394 = LDS16(wb1 + 19456);
  MFV(a10_5, w1387, x10_10);
  WAITC(63, 6);
  const v4u w1395 = LDS16(wb1 + 20480);
  MFV(a10_6, w1388, x10_10);
  WAITC(9, 6);
  const v4u w1396 = LDS16(wb1 + 21504);
  MFV(a10_0, w1389, x10_11);
  WAITC(63, 6);
  const v4u w1397 = LDS16(wb1 + 22528);
  MFV(a10_1, w1390, x10_11);
  WAITC(63, 6);
  const v4u w1398 = LDS16(wb1 + 23552);
  MFV(a10_2, w1391, x10_11);
  WAITC(63, 6);
  const v4u w1399 = LDS16(wb1 + 24576);
  MFV(a10_3, w1392, x10_11);
  WAITC(63, 6);
  const v4u w1400 = LDS16(wb1 + 25600);
  POLL_LD(pl1306, vp1, (gl + 3) * 256);
  MFV(a10_4, w1393, x10_11);
  WAITC(63, 6);
  const v4u w1401 = LDS16(wb1 + 26624);
  MFV(a10_5, w1394, x10_11);
  WAITC(63, 6);
  const v4u w1402 = LDS16(wb1 + 27648);
  MFV(a10_6, w1395, x10_11);
  WAITC(9, 6);
  const v4u w1403 = LDS16(wb1 + 28672);
  MFV(a10_0, w1396, x10_12);
  WAITC(63, 6);
  const v4u w1404 = LDS16(wb1 + 29696);
  MFV(a10_1, w1397, x10_12);
  WAITC(63, 6);
  const v4u w1405 = LDS16(wb1 + 30720);
  MFV(a10_2, w1398, x10_12);
  WAITC(63, 6);
  const v4u w1406 = LDS16(wb1 + 31744);
  MFV(a10_3, w1399, x10_12);
  WAITC(63, 6);
  const v4u w1407 = LDS16(wb1 + 32768);
  MFV(a10_4, w1400, x10_12);
  WAITC(63, 6);
  const v4u w1408 = LDS16(wb1 + 33792);
  MFV(a10_5, w1401, x10_12);
  WAITC(63, 6);
  const v4u w1409 = LDS16(wb1 + 34816);
  MFV(a10_6, w1402, x10_12);
  WAITC(8, 6);
  const v4u w1410 = LDS16(wb1 + 35840);
  MFV(a10_0, w1403, x10_13);
  WAITC(63, 6);
  const v4u w1411 = LDS16(wb1 + 36864);
  MFV(a10_1, w1404, x10_13);
  WAITC(63, 6);
  const v4u w1412 = LDS16(wb1 + 37888);
  MFV(a10_2, w1405, x10_13);
  WAITC(63, 6);
  const v4u w1413 = LDS16(wb1 + 38912);
  MFV(a10_3, w1406, x10_13);
  WAITC(63, 6);
  const v4u w1414 = LDS16(wb1 + 39936);
  MFV(a10_4, w1407, x10_13);
  WAITC(63, 6);
  const v4u w1415 = LDS16(wb1 + 40960);
  MFV(a10_5, w1408, x10_13);
  WAITC(63, 6);
  const v4u w1416 = LDS16(wb1 + 41984);
  MFV(a10_6, w1409, x10_13);
  WAITC(7, 6);
  const v4u w1417 = LDS16(wb1 + 43008);
  MFV(a10_0, w1410, x10_14);
  WAITC(63, 6);
  const v4u w1418 = LDS16(wb1 + 44032);
  MFV(a10_1, w1411, x10_14);
  WAITC(63, 6);
  const v4u w1419 = LDS16(wb1 + 45056);
  MFV(a10_2, w1412, x10_14);
  WAITC(63, 6);
  const v4u w1420 = LDS16(wb1 + 46080);
  MFV(a10_3, w1413, x10_14);
  WAITC(63, 6);
  const v4u w1421 = LDS16(wb1 + 47104);
  MFV(a10_4, w1414, x10_14);
  WAITC(63, 6);
  const v4u w1422 = LDS16(wb1 + 48128);
  MFV(a10_5, w1415, x10_14);
  WAITC(63, 6);
  const v4u w1423 = LDS16(wb1 + 49152);
  MFV(a10_6, w1416, x10_14);
  WAITC(6, 6);
  const v4u w1424 = LDS16(wb1 + 50176);
  MFV(a10_0, w1417, x10_15);
  WAITC(63, 6);
  const v4u w1425 = LDS16(wb1 + 51200);
  MFV(a10_1, w1418, x10_15);
  WAITC(63, 6);
  const v4u w1426 = LDS16(wb1 + 52224);
  MFV(a10_2, w1419, x10_15);
  WAITC(63, 6);
  const v4u w1427 = LDS16(wb1 + 53248);
  MFV(a10_3, w1420, x10_15);
  WAITC(63, 6);
  const v4u w1428 = LDS16(wb1 + 54272);
  MFV(a10_4, w1421, x10_15);
  WAITC(63, 6);
  const v4u w1429 = LDS16(wb1 + 55296);
  MFV(a10_5, w1422, x10_15);
  WAITC(63, 6);
  const v4u w1430 = LDS16(wb1 + 56320);
  MFV(a10_6, w1423, x10_15);
  WAITC(4, 6);
  const v4u w1431 = LDS16(wb1 + 57344);
  MFV(a10_0, w1424, x10_16);
  WAITC(0, 6);
  const v4u w1432 = LDS16(wb1 + 58368);
  POLL_CK(pl1306, vp1, (gl + 3) * 256, 16);
  MFV(a10_1, w1425, x10_16);
  WAITC(63, 6);
  const v4u w1433 = LDS16(wb1 + 59392);
  const v2u sp1_0 = ld8(ws, vsp + 0, spb1, SC1);
  MFV(a10_2, w1426, x10_16);
  WAITC(63, 6);
  const v4u w1434 = LDS16(wb1 + 60416);
  MFV(a10_3, w1427, x10_16);
  WAITC(63, 6);
  const v4u w1435 = LDS16(wb1 + 61440);
  MFV(a10_4, w1428, x10_16);
  WAITC(63, 6);
  const v4u w1436 = LDS16(wb1 + 62464);
  MFV(a10_5, w1429, x10_16);
  WAITC(63, 6);
  const v4u w1437 = LDS16(wb1 + 63488);
  const v2u sp1_1 = ld8(ws, vsp + 128, spb1, SC1);
  MFV(a10_6, w1430, x10_16);
  WAITC(63, 6);
  const v4u w1438 = LDS16(wb2 + 0);
  MFV(a10_0, w1431, x10_17);
  WAITC(63, 6);
  const v4u w1439 = LDS16(wb2 + 1024);
  MFV(a10_1, w1432, x10_17);
  WAITC(63, 6);
  const v4u w1440 = LDS16(wb2 + 2048);
  MFV(a10_2, w1433, x10_17);
  WAITC(63, 6);
  const v4u w1441 = LDS16(wb2 + 3072);
  const v2u sp1_2 = ld8(ws, vsp + 256, spb1, SC1);
  MFV(a10_3, w1434, x10_17);
  WAITC(63, 6);
  const v4u w1442 = LDS16(wb2 + 4096);
  MFV(a10_4, w1435, x10_17);
  WAITC(63, 6);
  const v4u w1443 = LDS16(wb2 + 5120);
  MFV(a10_5, w1436, x10_17);
  WAITC(63, 6);
  const v4u w1444 = LDS16(wb2 + 6144);
  MFV(a10_6, w1437, x10_17);
  WAITC(63, 5);
  const v4u w1445 = LDS16(wb2 + 7168);
  const v2u sp1_3 = ld8(ws, vsp + 384, spb1, SC1);
  MFV(a10_0, w1438, x10_18);
  int so1307; SADDS(so1307, xb11, xo0);
  const v4u w1446 = LDS16(wb2 + 8192);
  MFV(a10_1, w1439, x10_18);
  WAITC(63, 6);
  const v4u w1447 = LDS16(wb2 + 9216);
  MFV(a10_2, w1440, x10_18);
  WAITC(63, 6);
  const v4u w1448 = LDS16(wb2 + 10240);
  MFV(a10_3, w1441, x10_18);
  WAITC(63, 6);
  const v4u w1449 = LDS16(wb2 + 11264);
  v4u x11_0 = ld16(ws, vo0 + 0, so1307, SC1);
  MFV(a10_4, w1442, x10_18);
  WAITC(63, 6);
  const v4u w1450 = LDS16(wb2 + 12288);
  MFV(a10_5, w1443, x10_18);
  WAITC(63, 6);
  const v4u w1451 = LDS16(wb2 + 13312);
  MFV(a10_6, w1444, x10_18);
  WAITC(63, 6);
  MFV(a10_0, w1445, x10_19);
  WAITC(63, 5);
  v4u x11_1 = ld16(ws, vo0 + 1024, so1307, SC1);
  MFV(a10_1, w1446, x10_19);
  WAITC(63, 4);
  MFV(a10_2, w1447, x10_19);
  WAITC(63, 3);
  MFV(a10_3, w1448, x10_19);
  WAITC(63, 2);
  MFV(a10_4, w1449, x10_19);
  WAITC(63, 1);
  v4u x11_2 = ld16(ws, vo0 + 2048, so1307, SC1);
  MFV(a10_5, w1450, x10_19);
  WAITC(63, 0);
  MFV(a10_6, w1451, x10_19);
  STAMP(20);
  const f32x4 cs1452 = LDSF4(cbp + 0);
  const f32x4 bb1459 = LDSF4(cbp + 64);
  const f32x4 cs1453 = LDSF4(cbp + 128);
  const f32x4 bb1460 = LDSF4(cbp + 192);
  NOP8(); TIE(a10_0); TIE(a10_1); TIE(a10_2); TIE(a10_3); TIE(a10_4); TIE(a10_5); TIE(a10_6);
  const float rl10 = rs10 * 1.4426950408889634f, nl10 = nm10 * 1.4426950408889634f; const f32x2 r210 = {rl10, rl10}, n210 = {nl10, nl10}; SB();
  WAITC(63, 2);
  const f32x2 m1466 = PFMA(LO2(cs1452), n210, LO2(bb1459)); SB();
  const f32x2 m1478 = PFMA(HI2(cs1452), n210, HI2(bb1459)); SB();
  const f32x2 m1468 = PFMA(LO2(a10_0), r210, m1466); SB();
  const f32x2 m1480 = PFMA(HI2(a10_0), r210, m1478); SB();
  const float m1469 = __builtin_amdgcn_exp2f(m1468[0]); SB();
  v4u x11_3 = ld16(ws, vo0 + 3072, so1307, SC1);
  const float m1470 = __builtin_amdgcn_exp2f(m1468[1]); SB();
  const float m1481 = __builtin_amdgcn_exp2f(m1480[0]); SB();
  const float m1482 = __builtin_amdgcn_exp2f(m1480[1]); SB();
  int so1308; SADDS(so1308, xb11, xo1);
  v4u x11_4 = ld16(ws, vo0 + 0, so1308, SC1);
  const f32x4 cs1454 = LDSF4(cbp + 256);
  const f32x4 bb1461 = LDSF4(cbp + 320);
  const f32x2 m1471 = kone + f32x2{m1469, m1470}; SB();
  const f32x2 m1483 = kone + f32x2{m1481, m1482}; SB();
  const f32x2 m1472 = PFMA(m1471, m1471, kone); SB();
  v4u x11_5 = ld16(ws, vo0 + 1024, so1308, SC1);
  const f32x2 m1484 = PFMA(m1483, m1483, kone); SB();
  const float m1473 = __builtin_amdgcn_rcpf(m1472[0]); SB();
  const float m1474 = __builtin_amdgcn_rcpf(m1472[1]); SB();
  v4u x11_6 = ld16(ws, vo0 + 2048, so1308, SC1);
  const float m1485 = __builtin_amdgcn_rcpf(m1484[0]); SB();
  const float m1486 = __builtin_amdgcn_rcpf(m1484[1]); SB();
  const f32x2 m1475 = PFMA((f32x2{m1473, m1474}), km2, kln2); SB();
  v4u x11_7 = ld16(ws, vo0 + 3072, so1308, SC1);
  const f32x2 m1487 = PFMA((f32x2{m1485, m1486}), km2, kln2); SB();
  const f32x2 m1476 = m1468 * m1475; SB();
  const f32x2 m1488 = m1480 * m1487; SB();
  const unsigned m1477 = pk(m1476[0], m1476[1]); SB();
  const unsigned m1489 = pk(m1488[0], m1488[1]); SB();
  WAITC(63, 2);
  const f32x2 m1490 = PFMA(LO2(cs1453), n210, LO2(bb1460)); SB();
  const f32x2 m1502 = PFMA(HI2(cs1453), n210, HI2(bb1460)); SB();
  const f32x2 m1492 = PFMA(LO2(a10_1), r210, m1490); SB();
  const f32x2 m1504 = PFMA(HI2(a10_1), r210, m1502); SB();
  const float m1493 = __builtin_amdgcn_exp2f(m1492[0]); SB();
  const float m1494 = __builtin_amdgcn_exp2f(m1492[1]); SB();
  const float m1505 = __builtin_amdgcn_exp2f(m1504[0]); SB();
  const float m1506 = __builtin_amdgcn_exp2f(m1504[1]); SB();
  const f32x4 cs1455 = LDSF4(cbp + 384);
  const f32x4 bb1462 = LDSF4(cbp + 448);
  const f32x2 m1495 = kone + f32x2{m1493, m1494}; SB();
  const f32x2 m1507 = kone + f32x2{m1505, m1506}; SB();
  const f32x2 m1496 = PFMA(m1495, m1495, kone); SB();
  const f32x2 m1508 = PFMA(m1507, m1507, kone); SB();
  const float m1497 = __builtin_amdgcn_rcpf(m1496[0]); SB();
  const float m1498 = __builtin_amdgcn_rcpf(m1496[1]); SB();
  const float m1509 = __builtin_amdgcn_rcpf(m1508[0]); SB();
  const float m1510 = __builtin_amdgcn_rcpf(m1508[1]); SB();
  const f32x2 m1499 = PFMA((f32x2{m1497, m1498}), km2, kln2); SB();
  const f32x2 m1511 = PFMA((f32x2{m1509, m1510}), km2, kln2); SB();
  const f32x2 m1500 = m1492 * m1499; SB();
  const f32x2 m1512 = m1504 * m1511; SB();
  const unsigned m1501 = pk(m1500[0], m1500[1]); SB();
  const unsigned m1513 = pk(m1512[0], m1512[1]); SB();
  st16(v4u{m1477, m1489, m1501, m1513}, ws, vo0 + 0, hs10);
  WAITC(63, 2);
  const f32x2 m1514 = PFMA(LO2(cs1454), n210, LO2(bb1461)); SB();
  const f32x2 m1526 = PFMA(HI2(cs1454), n210, HI2(bb1461)); SB();
  const f32x2 m1516 = PFMA(LO2(a10_2), r210, m1514); SB();
  const f32x2 m1528 = PFMA(HI2(a10_2), r210, m1526); SB();
  const float m1517 = __builtin_amdgcn_exp2f(m1516[0]); SB();
  const float m1518 = __builtin_amdgcn_exp2f(m1516[1]); SB();
  const float m1529 = __builtin_amdgcn_exp2f(m1528[0]); SB();
  const float m1530 = __builtin_amdgcn_exp2f(m1528[1]); SB();
  const f32x4 cs1456 = LDSF4(cbp + 512);
  const f32x4 bb1463 = LDSF4(cbp + 576);
  const f32x2 m1519 = kone + f32x2{m1517, m1518}; SB();
  const f32x2 m1531 = kone + f32x2{m1529, m1530}; SB();
  const f32x2 m1520 = PFMA(m1519, m1519, kone); SB();
  const f32x2 m1532 = PFMA(m1531, m1531, kone); SB();
  const float m1521 = __builtin_amdgcn_rcpf(m1520[0]); SB();
  const float m1522 = __builtin_amdgcn_rcpf(m1520[1]); SB();
  const float m1533 = __builtin_amdgcn_rcpf(m1532[0]); SB();
  const float m1534 = __builtin_amdgcn_rcpf(m1532[1]); SB();
  const f32x2 m1523 = PFMA((f32x2{m1521, m1522}), km2, kln2); SB();
  const f32x2 m1535 = PFMA((f32x2{m1533, m1534}), km2, kln2); SB();
  const f32x2 m1524 = m1516 * m1523; SB();
  const f32x2 m1536 = m1528 * m1535; SB();
  const unsigned m1525 = pk(m1524[0], m1524[1]); SB();
  const unsigned m1537 = pk(m1536[0], m1536[1]); SB();
  WAITC(63, 2);
  const f32x2 m1538 = PFMA(LO2(cs1455), n210, LO2(bb1462)); SB();
  const f32x2 m1550 = PFMA(HI2(cs1455), n210, HI2(bb1462)); SB();
  const f32x2 m1540 = PFMA(LO2(a10_3), r210, m1538); SB();
  const f32x2 m1552 = PFMA(HI2(a10_3), r210, m1550); SB();
  const float m1541 = __builtin_amdgcn_exp2f(m1540[0]); SB();
  const float m1542 = __builtin_amdgcn_exp2f(m1540[1]); SB();
  const float m1553 = __builtin_amdgcn_exp2f(m1552[0]); SB();
  const float m1554 = __builtin_amdgcn_exp2f(m1552[1]); SB();
  const f32x4 cs1457 = LDSF4(cbp + 640);
  const f32x4 bb1464 = LDSF4(cbp + 704);
  const f32x2 m1543 = kone + f32x2{m1541, m1542}; SB();
  const f32x2 m1555 = kone + f32x2{m1553, m1554}; SB();
  const f32x2 m1544 = PFMA(m1543, m1543, kone); SB();
  const f32x2 m1556 = PFMA(m1555, m1555, kone); SB();
  const float m1545 = __builtin_amdgcn_rcpf(m1544[0]); SB();
  const float m1546 = __builtin_amdgcn_rcpf(m1544[1]); SB();
  const float m1557 = __builtin_amdgcn_rcpf(m1556[0]); SB();
  const float m1558 = __builtin_amdgcn_rcpf(m1556[1]); SB();
  const f32x2 m1547 = PFMA((f32x2{m1545, m1546}), km2, kln2); SB();
  const f32x2 m1559 = PFMA((f32x2{m1557, m1558}), km2, kln2); SB();
  const f32x2 m1548 = m1540 * m1547; SB();
  const f32x2 m1560 = m1552 * m1559; SB();
  const unsigned m1549 = pk(m1548[0], m1548[1]); SB();
  const unsigned m1561 = pk(m1560[0], m1560[1]); SB();
  st16(v4u{m1525, m1537, m1549, m1561}, ws, vo0 + 1024, hs10);
  WAITC(63, 2);
  const f32x2 m1562 = PFMA(LO2(cs1456), n210, LO2(bb1463)); SB();
  const f32x2 m1574 = PFMA(HI2(cs1456), n210, HI2(bb1463)); SB();
  const f32x2 m1564 = PFMA(LO2(a10_4), r210, m1562); SB();
  const f32x2 m1576 = PFMA(HI2(a10_4), r210, m1574); SB();
  const float m1565 = __builtin_amdgcn_exp2f(m1564[0]); SB();
  const float m1566 = __builtin_amdgcn_exp2f(m1564[1]); SB();
  const float m1577 = __builtin_amdgcn_exp2f(m1576[0]); SB();
  const float m1578 = __builtin_amdgcn_exp2f(m1576[1]); SB();
  const f32x4 cs1458 = LDSF4(cbp + 768);
  const f32x4 bb1465 = LDSF4(cbp + 832);
  const f32x2 m1567 = kone + f32x2{m1565, m1566}; SB();
  const f32x2 m1579 = kone + f32x2{m1577, m1578}; SB();
  const f32x2 m1568 = PFMA(m1567, m1567, kone); SB();
  const f32x2 m1580 = PFMA(m1579, m1579, kone); SB();
  const float m1569 = __builtin_amdgcn_rcpf(m1568[0]); SB();
  const float m1570 = __builtin_amdgcn_rcpf(m1568[1]); SB();
  const float m1581 = __builtin_amdgcn_rcpf(m1580[0]); SB();
  const float m1582 = __builtin_amdgcn_rcpf(m1580[1]); SB();
  const f32x2 m1571 = PFMA((f32x2{m1569, m1570}), km2, kln2); SB();
  const f32x2 m1583 = PFMA((f32x2{m1581, m1582}), km2, kln2); SB();
  const f32x2 m1572 = m1564 * m1571; SB();
  const f32x2 m1584 = m1576 * m1583; SB();
  const unsigned m1573 = pk(m1572[0], m1572[1]); SB();
  const unsigned m1585 = pk(m1584[0], m1584[1]); SB();
  WAITC(63, 2);
  const f32x2 m1586 = PFMA(LO2(cs1457), n210, LO2(bb1464)); SB();
  const f32x2 m1598 = PFMA(HI2(cs1457), n210, HI2(bb1464)); SB();
  const f32x2 m1588 = PFMA(LO2(a10_5), r210, m1586); SB();
  const f32x2 m1600 = PFMA(HI2(a10_5), r210, m1598); SB();
  const float m1589 = __builtin_amdgcn_exp2f(m1588[0]); SB();
  const float m1590 = __builtin_amdgcn_exp2f(m1588[1]); SB();
  const float m1601 = __builtin_amdgcn_exp2f(m1600[0]); SB();
  const float m1602 = __builtin_amdgcn_exp2f(m1600[1]); SB();
  const f32x2 m1591 = kone + f32x2{m1589, m1590}; SB();
  const f32x2 m1603 = kone + f32x2{m1601, m1602}; SB();
  const f32x2 m1592 = PFMA(m1591, m1591, kone); SB();
  const f32x2 m1604 = PFMA(m1603, m1603, kone); SB();
  const float m1593 = __builtin_amdgcn_rcpf(m1592[0]); SB();
  const float m1594 = __builtin_amdgcn_rcpf(m1592[1]); SB();
  const float m1605 = __builtin_amdgcn_rcpf(m1604[0]); SB();
  const float m1606 = __builtin_amdgcn_rcpf(m1604[1]); SB();
  const f32x2 m1595 = PFMA((f32x2{m1593, m1594}), km2, kln2); SB();
  const f32x2 m1607 = PFMA((f32x2{m1605, m1606}), km2, kln2); SB();
  const f32x2 m1596 = m1588 * m1595; SB();
  const f32x2 m1608 = m1600 * m1607; SB();
  const unsigned m1597 = pk(m1596[0], m1596[1]); SB();
  const unsigned m1609 = pk(m1608[0], m1608[1]); SB();
  st16(v4u{m1573, m1585, m1597, m1609}, ws, vo0 + 2048, hs10);
  WAITC(63, 0);
  const f32x2 m1610 = PFMA(LO2(cs1458), n210, LO2(bb1465)); SB();
  const f32x2 m1622 = PFMA(HI2(cs1458), n210, HI2(bb1465)); SB();
  const f32x2 m1612 = PFMA(LO2(a10_6), r210, m1610); SB();
  const f32x2 m1624 = PFMA(HI2(a10_6), r210, m1622); SB();
  const float m1613 = __builtin_amdgcn_exp2f(m1612[0]); SB();
  const float m1614 = __builtin_amdgcn_exp2f(m1612[1]); SB();
  const float m1625 = __builtin_amdgcn_exp2f(m1624[0]); SB();
  const float m1626 = __builtin_amdgcn_exp2f(m1624[1]); SB();
  const f32x2 m1615 = kone + f32x2{m1613, m1614}; SB();
  const f32x2 m1627 = kone + f32x2{m1625, m1626}; SB();
  const f32x2 m1616 = PFMA(m1615, m1615, kone); SB();
  const f32x2 m1628 = PFMA(m1627, m1627, kone); SB();
  const float m1617 = __builtin_amdgcn_rcpf(m1616[0]); SB();
  const float m1618 = __builtin_amdgcn_rcpf(m1616[1]); SB();
  const float m1629 = __builtin_amdgcn_rcpf(m1628[0]); SB();
  const float m1630 = __builtin_amdgcn_rcpf(m1628[1]); SB();
  const f32x2 m1619 = PFMA((f32x2{m1617, m1618}), km2, kln2); SB();
  const f32x2 m1631 = PFMA((f32x2{m1629, m1630}), km2, kln2); SB();
  const f32x2 m1620 = m1612 * m1619; SB();
  const f32x2 m1632 = m1624 * m1631; SB();
  const unsigned m1621 = pk(m1620[0], m1620[1]); SB();
  const v4u w1663 = LDS16(wb0 + 0);
  const v4u w1664 = LDS16(wb0 + 1024);
  const v4u w1665 = LDS16(wb0 + 2048);
  const v4u w1666 = LDS16(wb0 + 3072);
  const v4u w1667 = LDS16(wb0 + 4096);
  const v4u w1668 = LDS16(wb0 + 5120);
  const v4u w1669 = LDS16(wb0 + 6144);
  const v4u w1670 = LDS16(wb0 + 7168);
  const unsigned m1633 = pk(m1632[0], m1632[1]); SB();
  st8(v2u{m1621, m1633}, ws, vl8, ht10);
  WAITC(12, 15);
  float rs11, nm11; lnstat(xsum_g((__uint_as_float(sp1_0.x) + __uint_as_float(sp1_1.x)) + (__uint_as_float(sp1_2.x) + __uint_as_float(sp1_3.x))), xsum_g((__uint_as_float(sp1_0.y) + __uint_as_float(sp1_1.y)) + (__uint_as_float(sp1_2.y) + __uint_as_float(sp1_3.y))), rs11, nm11); TIE(rs11); TIE(nm11);
  STAMP(21);
  WAITC(11, 7);
  f32x4 a11_0; MFV0(a11_0, w1663, x11_0);
  int so1309; SADDS(so1309, xb11, xo2);
  const v4u w1671 = LDS16(wb0 + 8192);
  v4u x11_8 = ld16(ws, vo0 + 0, so1309, SC1);
  WAITC(63, 7);
  f32x4 a11_1; MFV0(a11_1, w1664, x11_0);
  WAITC(63, 6);
  const v4u w1672 = LDS16(wb0 + 9216);
  f32x4 a11_2; MFV0(a11_2, w1665, x11_0);
  WAITC(63, 6);
  const v4u w1673 = LDS16(wb0 + 10240);
  f32x4 a11_3; MFV0(a11_3, w1666, x11_0);
  WAITC(63, 6);
  const v4u w1674 = LDS16(wb0 + 11264);
  f32x4 a11_4; MFV0(a11_4, w1667, x11_0);
  WAITC(63, 6);
  const v4u w1675 = LDS16(wb0 + 12288);
  v4u x11_9 = ld16(ws, vo0 + 1024, so1309, SC1);
  f32x4 a11_5; MFV0(a11_5, w1668, x11_0);
  WAITC(63, 6);
  const v4u w1676 = LDS16(wb0 + 13312);
  f32x4 a11_6; MFV0(a11_6, w1669, x11_0);
  WAITC(12, 6);
  const v4u w1677 = LDS16(wb0 + 14336);
  MFV(a11_0, w1670, x11_1);
  WAITC(63, 6);
  const v4u w1678 = LDS16(wb0 + 15360);
  MFV(a11_1, w1671, x11_1);
  WAITC(63, 6);
  const v4u w1679 = LDS16(wb0 + 16384);
  v4u x11_10 = ld16(ws, vo0 + 2048, so1309, SC1);
  MFV(a11_2, w1672, x11_1);
  WAITC(63, 6);
  const v4u w1680 = LDS16(wb0 + 17408);
  MFV(a11_3, w1673, x11_1);
  WAITC(63, 6);
  const v4u w1681 = LDS16(wb0 + 18432);
  MFV(a11_4, w1674, x11_1);
  WAITC(63, 6);
  const v4u w1682 = LDS16(wb0 + 19456);
  MFV(a11_5, w1675, x11_1);
  WAITC(12, 5);
  const v4u w1683 = LDS16(wb0 + 20480);
  v4u x11_11 = ld16(ws, vo0 + 3072, so1309, SC1);
  MFV(a11_6, w1676, x11_1);
  int so1310; SADDS(so1310, xb11, xo3);
  const v4u w1684 = LDS16(wb0 + 21504);
  MFV(a11_0, w1677, x11_2);
  WAITC(63, 6);
  const v4u w1685 = LDS16(wb0 + 22528);
  MFV(a11_1, w1678, x11_2);
  WAITC(63, 6);
  const v4u w1686 = LDS16(wb0 + 23552);
  MFV(a11_2, w1679, x11_2);
  WAITC(63, 6);
  const v4u w1687 = LDS16(wb0 + 24576);
  v4u x11_12 = ld16(ws, vo0 + 0, so1310, SC1);
  MFV(a11_3, w1680, x11_2);
  WAITC(63, 6);
  const v4u w1688 = LDS16(wb0 + 25600);
  MFV(a11_4, w1681, x11_2);
  WAITC(63, 6);
  const v4u w1689 = LDS16(wb0 + 26624);
  MFV(a11_5, w1682, x11_2);
  WAITC(63, 6);
  const v4u w1690 = LDS16(wb0 + 27648);
  MFV(a11_6, w1683, x11_2);
  WAITC(13, 6);
  const v4u w1691 = LDS16(wb0 + 28672);
  v4u x11_13 = ld16(ws, vo0 + 1024, so1310, SC1);
  MFV(a11_0, w1684, x11_3);
  WAITC(63, 6);
  const v4u w1692 = LDS16(wb0 + 29696);
  MFV(a11_1, w1685, x11_3);
  WAITC(63, 6);
  const v4u w1693 = LDS16(wb0 + 30720);
  MFV(a11_2, w1686, x11_3);
  WAITC(63, 6);
  const v4u w1694 = LDS16(wb0 + 31744);
  MFV(a11_3, w1687, x11_3);
  WAITC(63, 6);
  const v4u w1695 = LDS16(wb0 + 32768);
  v4u x11_14 = ld16(ws, vo0 + 2048, so1310, SC1);
  MFV(a11_4, w1688, x11_3);
  WAITC(63, 6);
  const v4u w1696 = LDS16(wb0 + 33792);
  MFV(a11_5, w1689, x11_3);
  WAITC(63, 6);
  const v4u w1697 = LDS16(wb0 + 34816);
  MFV(a11_6, w1690, x11_3);
  WAITC(14, 6);
  const v4u w1698 = LDS16(wb0 + 35840);
  MFV(a11_0, w1691, x11_4);
  WAITC(63, 5);
  const v4u w1699 = LDS16(wb0 + 36864);
  v4u x11_15 = ld16(ws, vo0 + 3072, so1310, SC1);
  MFV(a11_1, w1692, x11_4);
  int so1311; SADDS(so1311, xb11, xo4);
  const v4u w1700 = LDS16(wb0 + 37888);
  MFV(a11_2, w1693, x11_4);
  WAITC(63, 6);
  const v4u w1701 = LDS16(wb0 + 38912);
  MFV(a11_3, w1694, x11_4);
  WAITC(63, 6);
  const v4u w1702 = LDS16(wb0 + 39936);
  MFV(a11_4, w1695, x11_4);
  WAITC(63, 6);
  const v4u w1703 = LDS16(wb0 + 40960);
  v4u x11_16 = ld16(ws, vo0 + 0, so1311, SC1);
  MFV(a11_5, w1696, x11_4);
  WAITC(63, 6);
  const v4u w1704 = LDS16(wb0 + 41984);
  MFV(a11_6, w1697, x11_4);
  WAITC(15, 6);
  const v4u w1705 = LDS16(wb0 + 43008);
  MFV(a11_0, w1698, x11_5);
  WAITC(63, 6);
  const v4u w1706 = LDS16(wb0 + 44032);
  MFV(a11_1, w1699, x11_5);
  WAITC(63, 6);
  const v4u w1707 = LDS16(wb0 + 45056);
  v4u x11_17 = ld16(ws, vo0 + 1024, so1311, SC1);
  MFV(a11_2, w1700, x11_5);
  WAITC(63, 6);
  const v4u w1708 = LDS16(wb0 + 46080);
  MFV(a11_3, w1701, x11_5);
  WAITC(63, 6);
  const v4u w1709 = LDS16(wb0 + 47104);
  MFV(a11_4, w1702, x11_5);
  WAITC(63, 6);
  const v4u w1710 = LDS16(wb0 + 48128);
  MFV(a11_5, w1703, x11_5);
  WAITC(63, 6);
  const v4u w1711 = LDS16(wb0 + 49152);
  MFV(a11_6, w1704, x11_5);
  WAITC(15, 6);
  const v4u w1712 = LDS16(wb0 + 50176);
  MFV(a11_0, w1705, x11_6);
  WAITC(63, 6);
  const v4u w1713 = LDS16(wb0 + 51200);
  MFV(a11_1, w1706, x11_6);
  WAITC(63, 6);
  const v4u w1714 = LDS16(wb0 + 52224);
  MFV(a11_2, w1707, x11_6);
  WAITC(63, 6);
  const v4u w1715 = LDS16(wb0 + 53248);
  MFV(a11_3, w1708, x11_6);
  WAITC(63, 6);
  const v4u w1716 = LDS16(wb0 + 54272);
  MFV(a11_4, w1709, x11_6);
  WAITC(63, 6);
  const v4u w1717 = LDS16(wb0 + 55296);
  MFV(a11_5, w1710, x11_6);
  WAITC(63, 6);
  const v4u w1718 = LDS16(wb0 + 56320);
  MFV(a11_6, w1711, x11_6);
  WAITC(14, 6);
  const v4u w1719 = LDS16(wb0 + 57344);
  MFV(a11_0, w1712, x11_7);
  WAITC(63, 6);
  const v4u w1720 = LDS16(wb0 + 58368);
  MFV(a11_1, w1713, x11_7);
  WAITC(63, 6);
  const v4u w1721 = LDS16(wb0 + 59392);
  DRAINBAR(10);
  MFV(a11_2, w1714, x11_7);
  WAITC(63, 6);
  const v4u w1722 = LDS16(wb0 + 60416);
  ARRIVE(va1, (gl + 4) * 256);
  MFV(a11_3, w1715, x11_7);
  WAITC(63, 6);
  const v4u w1723 = LDS16(wb0 + 61440);
  MFV(a11_4, w1716, x11_7);
  WAITC(63, 6);
  const v4u w1724 = LDS16(wb0 + 62464);
  MFV(a11_5, w1717, x11_7);
  WAITC(63, 6);
  const v4u w1725 = LDS16(wb0 + 63488);
  MFV(a11_6, w1718, x11_7);
  WAITC(10, 6);
  const v4u w1726 = LDS16(wb1 + 0);
  v4u x11_18 = ld16(ws, vo0 + 2048, so1311, SC1);
  MFV(a11_0, w1719, x11_8);
  WAITC(63, 6);
  const v4u w1727 = LDS16(wb1 + 1024);
  MFV(a11_1, w1720, x11_8);
  WAITC(63, 6);
  const v4u w1728 = LDS16(wb1 + 2048);
  MFV(a11_2, w1721, x11_8);
  WAITC(63, 6);
  const v4u w1729 = LDS16(wb1 + 3072);
  MFV(a11_3, w1722, x11_8);
  WAITC(63, 6);
  const v4u w1730 = LDS16(wb1 + 4096);
  v4u x11_19 = ld16(ws, vo0 + 3072, so1311, SC1);
  MFV(a11_4, w1723, x11_8);
  WAITC(63, 6);
  const v4u w1731 = LDS16(wb1 + 5120);
  MFV(a11_5, w1724, x11_8);
  WAITC(63, 6);
  const v4u w1732 = LDS16(wb1 + 6144);
  MFV(a11_6, w1725, x11_8);
  WAITC(11, 6);
  const v4u w1733 = LDS16(wb1 + 7168);
  MFV(a11_0, w1726, x11_9);
  WAITC(63, 6);
  const v4u w1734 = LDS16(wb1 + 8192);
  MFV(a11_1, w1727, x11_9);
  WAITC(63, 6);
  const v4u w1735 = LDS16(wb1 + 9216);
  MFV(a11_2, w1728, x11_9);
  WAITC(63, 6);
  const v4u w1736 = LDS16(wb1 + 10240);
  MFV(a11_3, w1729, x11_9);
  WAITC(63, 6);
  const v4u w1737 = LDS16(wb1 + 11264);
  MFV(a11_4, w1730, x11_9);
  WAITC(63, 6);
  const v4u w1738 = LDS16(wb1 + 12288);
  MFV(a11_5, w1731, x11_9);
  WAITC(63, 6);
  const v4u w1739 = LDS16(wb1 + 13312);
  MFV(a11_6, w1732, x11_9);
  WAITC(10, 6);
  const v4u w1740 = LDS16(wb1 + 14336);
  MFV(a11_0, w1733, x11_10);
  WAITC(63, 6);
  const v4u w1741 = LDS16(wb1 + 15360);
  MFV(a11_1, w1734, x11_10);
  WAITC(63, 6);
  const v4u w1742 = LDS16(wb1 + 16384);
  MFV(a11_2, w1735, x11_10);
  WAITC(63, 6);
  const v4u w1743 = LDS16(wb1 + 17408);
  MFV(a11_3, w1736, x11_10);
  WAITC(63, 6);
  const v4u w1744 = LDS16(wb1 + 18432);
  MFV(a11_4, w1737, x11_10);
  WAITC(63, 6);
  const v4u w1745 = LDS16(wb1 + 19456);
  MFV(a11_5, w1738, x11_10);
  WAITC(63, 6);
  const v4u w1746 = LDS16(wb1 + 20480);
  MFV(a11_6, w1739, x11_10);
  WAITC(9, 6);
  const v4u w1747 = LDS16(wb1 + 21504);
  MFV(a11_0, w1740, x11_11);
  WAITC(63, 6);
  const v4u w1748 = LDS16(wb1 + 22528);
  MFV(a11_1, w1741, x11_11);
  WAITC(63, 6);
  const v4u w1749 = LDS16(wb1 + 23552);
  MFV(a11_2, w1742, x11_11);
  WAITC(63, 6);
  const v4u w1750 = LDS16(wb1 + 24576);
  MFV(a11_3, w1743, x11_11);
  WAITC(63, 6);
  const v4u w1751 = LDS16(wb1 + 25600);
  POLL_LD(pl1634, vp1, (gl2 + 4) * 256);
  MFV(a11_4, w1744, x11_11);
  WAITC(63, 6);
  const v4u w1752 = LDS16(wb1 + 26624);
  MFV(a11_5, w1745, x11_11);
  WAITC(63, 6);
  const v4u w1753 = LDS16(wb1 + 27648);
  MFV(a11_6, w1746, x11_11);
  WAITC(9, 6);
  const v4u w1754 = LDS16(wb1 + 28672);
  MFV(a11_0, w1747, x11_12);
  WAITC(63, 6);
  const v4u w1755 = LDS16(wb1 + 29696);
  MFV(a11_1, w1748, x11_12);
  WAITC(63, 6);
  const v4u w1756 = LDS16(wb1 + 30720);
  MFV(a11_2, w1749, x11_12);
  WAITC(63, 6);
  const v4u w1757 = LDS16(wb1 + 31744);
  MFV(a11_3, w1750, x11_12);
  WAITC(63, 6);
  const v4u w1758 = LDS16(wb1 + 32768);
  MFV(a11_4, w1751, x11_12);
  WAITC(63, 6);
  const v4u w1759 = LDS16(wb1 + 33792);
  MFV(a11_5, w1752, x11_12);
  WAITC(63, 6);
  const v4u w1760 = LDS16(wb1 + 34816);
  MFV(a11_6, w1753, x11_12);
  WAITC(8, 6);
  const v4u w1761 = LDS16(wb1 + 35840);
  MFV(a11_0, w1754, x11_13);
  WAITC(63, 6);
  const v4u w1762 = LDS16(wb1 + 36864);
  MFV(a11_1, w1755, x11_13);
  WAITC(63, 6);
  const v4u w1763 = LDS16(wb1 + 37888);
  MFV(a11_2, w1756, x11_13);
  WAITC(63, 6);
  const v4u w1764 = LDS16(wb1 + 38912);
  MFV(a11_3, w1757, x11_13);
  WAITC(63, 6);
  const v4u w1765 = LDS16(wb1 + 39936);
  MFV(a11_4, w1758, x11_13);
  WAITC(63, 6);
  const v4u w1766 = LDS16(wb1 + 40960);
  MFV(a11_5, w1759, x11_13);
  WAITC(63, 6);
  const v4u w1767 = LDS16(wb1 + 41984);
  MFV(a11_6, w1760, x11_13);
  WAITC(7, 6);
  const v4u w1768 = LDS16(wb1 + 43008);
  MFV(a11_0, w1761, x11_14);
  WAITC(63, 6);
  const v4u w1769 = LDS16(wb1 + 44032);
  MFV(a11_1, w1762, x11_14);
  WAITC(63, 6);
  const v4u w1770 = LDS16(wb1 + 45056);
  MFV(a11_2, w1763, x11_14);
  WAITC(63, 6);
  const v4u w1771 = LDS16(wb1 + 46080);
  MFV(a11_3, w1764, x11_14);
  WAITC(63, 6);
  const v4u w1772 = LDS16(wb1 + 47104);
  MFV(a11_4, w1765, x11_14);
  WAITC(63, 6);
  const v4u w1773 = LDS16(wb1 + 48128);
  MFV(a11_5, w1766, x11_14);
  WAITC(63, 6);
  const v4u w1774 = LDS16(wb1 + 49152);
  MFV(a11_6, w1767, x11_14);
  WAITC(6, 6);
  const v4u w1775 = LDS16(wb1 + 50176);
  MFV(a11_0, w1768, x11_15);
  WAITC(63, 6);
  const v4u w1776 = LDS16(wb1 + 51200);
  MFV(a11_1, w1769, x11_15);
  WAITC(63, 6);
  const v4u w1777 = LDS16(wb1 + 52224);
  MFV(a11_2, w1770, x11_15);
  WAITC(63, 6);
  const v4u w1778 = LDS16(wb1 + 53248);
  MFV(a11_3, w1771, x11_15);
  WAITC(63, 6);
  const v4u w1779 = LDS16(wb1 + 54272);
  MFV(a11_4, w1772, x11_15);
  WAITC(63, 6);
  const v4u w1780 = LDS16(wb1 + 55296);
  MFV(a11_5, w1773, x11_15);
  WAITC(63, 6);
  const v4u w1781 = LDS16(wb1 + 56320);
  MFV(a11_6, w1774, x11_15);
  WAITC(5, 6);
  const v4u w1782 = LDS16(wb1 + 57344);
  MFV(a11_0, w1775, x11_16);
  WAITC(0, 4);
  const v4u w1783 = LDS16(wb1 + 58368);
  POLL_CK(pl1634, vp1, (gl2 + 4) * 256, 16);
  MFV(a11_1, w1776, x11_16);
  int so1635; SADDS(so1635, hb100, ko0);
  const v4u w1784 = LDS16(wb1 + 59392);
  v4u h100_0 = ld16(ws, vo0 + 0, so1635, SC1);
  MFV(a11_2, w1777, x11_16);
  int so1636; SADDS(so1636, hb100, ko1);
  const v4u w1785 = LDS16(wb1 + 60416);
  MFV(a11_3, w1778, x11_16);
  WAITC(63, 6);
  const v4u w1786 = LDS16(wb1 + 61440);
  MFV(a11_4, w1779, x11_16);
  WAITC(63, 6);
  const v4u w1787 = LDS16(wb1 + 62464);
  MFV(a11_5, w1780, x11_16);
  WAITC(63, 5);
  const v4u w1788 = LDS16(wb1 + 63488);
  v4u h100_1 = ld16(ws, vo0 + 0, so1636, SC1);
  MFV(a11_6, w1781, x11_16);
  int so1637; SADDS(so1637, hb100, ko2);
  const v4u w1789 = LDS16(wb2 + 0);
  MFV(a11_0, w1782, x11_17);
  WAITC(63, 6);
  const v4u w1790 = LDS16(wb2 + 1024);
  MFV(a11_1, w1783, x11_17);
  WAITC(63, 6);
  const v4u w1791 = LDS16(wb2 + 2048);
  MFV(a11_2, w1784, x11_17);
  WAITC(63, 5);
  const v4u w1792 = LDS16(wb2 + 3072);
  v4u h100_2 = ld16(ws, vo0 + 0, so1637, SC1);
  MFV(a11_3, w1785, x11_17);
  int so1638; SADDS(so1638, hb100, ko3);
  const v4u w1793 = LDS16(wb2 + 4096);
  MFV(a11_4, w1786, x11_17);
  WAITC(63, 6);
  const v4u w1794 = LDS16(wb2 + 5120);
  MFV(a11_5, w1787, x11_17);
  WAITC(63, 6);
  const v4u w1795 = LDS16(wb2 + 6144);
  MFV(a11_6, w1788, x11_17);
  WAITC(63, 5);
  const v4u w1796 = LDS16(wb2 + 7168);
  v4u h100_3 = ld16(ws, vo0 + 0, so1638, SC1);
  MFV(a11_0, w1789, x11_18);
  int so1639; SADDS(so1639, hb100, ko4);
  const v4u w1797 = LDS16(wb2 + 8192);
  MFV(a11_1, w1790, x11_18);
  WAITC(63, 6);
  const v4u w1798 = LDS16(wb2 + 9216);
  MFV(a11_2, w1791, x11_18);
  WAITC(63, 6);
  const v4u w1799 = LDS16(wb2 + 10240);
  MFV(a11_3, w1792, x11_18);
  WAITC(63, 5);
  const v4u w1800 = LDS16(wb2 + 11264);
  v4u h100_4 = ld16(ws, vo0 + 0, so1639, SC1);
  MFV(a11_4, w1793, x11_18);
  int so1640; SADDS(so1640, hb100, ko5);
  const v4u w1801 = LDS16(wb2 + 12288);
  MFV(a11_5, w1794, x11_18);
  WAITC(63, 6);
  const v4u w1802 = LDS16(wb2 + 13312);
  MFV(a11_6, w1795, x11_18);
  WAITC(63, 6);
  MFV(a11_0, w1796, x11_19);
  WAITC(63, 5);
  v4u h100_5 = ld16(ws, vo0 + 0, so1640, SC1);
  MFV(a11_1, w1797, x11_19);
  WAITC(63, 4);
  MFV(a11_2, w1798, x11_19);
  WAITC(63, 3);
  MFV(a11_3, w1799, x11_19);
  WAITC(63, 2);
  MFV(a11_4, w1800, x11_19);
  WAITC(63, 1);
  MFV(a11_5, w1801, x11_19);
  WAITC(63, 0);
  MFV(a11_6, w1802, x11_19);
  STAMP(22);
  const f32x4 cs1803 = LDSF4(cbp + 0);
  const f32x4 bb1810 = LDSF4(cbp + 64);
  const f32x4 cs1804 = LDSF4(cbp + 128);
  const f32x4 bb1811 = LDSF4(cbp + 192);
  NOP8(); TIE(a11_0); TIE(a11_1); TIE(a11_2); TIE(a11_3); TIE(a11_4); TIE(a11_5); TIE(a11_6);
  const float rl11 = rs11 * 1.4426950408889634f, nl11 = nm11 * 1.4426950408889634f; const f32x2 r211 = {rl11, rl11}, n211 = {nl11, nl11}; SB();
  WAITC(63, 2);
  const f32x2 m1817 = PFMA(LO2(cs1803), n211, LO2(bb1810)); SB();
  const f32x2 m1829 = PFMA(HI2(cs1803), n211, HI2(bb1810)); SB();
  const f32x2 m1819 = PFMA(LO2(a11_0), r211, m1817); SB();
  const f32x2 m1831 = PFMA(HI2(a11_0), r211, m1829); SB();
  const float m1820 = __builtin_amdgcn_exp2f(m1819[0]); SB();
  int so1641; SADDS(so1641, hb100, ko6);
  v4u h100_6 = ld16(ws, vo0 + 0, so1641, SC1);
  const float m1821 = __builtin_amdgcn_exp2f(m1819[1]); SB();
  const float m1832 = __builtin_amdgcn_exp2f(m1831[0]); SB();
  const float m1833 = __builtin_amdgcn_exp2f(m1831[1]); SB();
  int so1642; SADDS(so1642, hb100, ko7);
  v4u h100_7 = ld16(ws, vo0 + 0, so1642, SC1);
  const f32x4 cs1805 = LDSF4(cbp + 256);
  const f32x4 bb1812 = LDSF4(cbp + 320);
  const f32x2 m1822 = kone + f32x2{m1820, m1821}; SB();
  const f32x2 m1834 = kone + f32x2{m1832, m1833}; SB();
  const f32x2 m1823 = PFMA(m1822, m1822, kone); SB();
  int so1643; SADDS(so1643, hb100, ko8);
  v4u h100_8 = ld16(ws, vo0 + 0, so1643, SC1);
  const f32x2 m1835 = PFMA(m1834, m1834, kone); SB();
  const float m1824 = __builtin_amdgcn_rcpf(m1823[0]); SB();
  const float m1825 = __builtin_amdgcn_rcpf(m1823[1]); SB();
  int so1644; SADDS(so1644, hb100, ko9);
  v4u h100_9 = ld16(ws, vo0 + 0, so1644, SC1);
  const float m1836 = __builtin_amdgcn_rcpf(m1835[0]); SB();
  const float m1837 = __builtin_amdgcn_rcpf(m1835[1]); SB();
  const f32x2 m1826 = PFMA((f32x2{m1824, m1825}), km2, kln2); SB();
  int so1645; SADDS(so1645, hb100, ko10);
  v4u h100_10 = ld16(ws, vo0 + 0, so1645, SC1);
  const f32x2 m1838 = PFMA((f32x2{m1836, m1837}), km2, kln2); SB();
  const f32x2 m1827 = m1819 * m1826; SB();
  const f32x2 m1839 = m1831 * m1838; SB();
  const unsigned m1828 = pk(m1827[0], m1827[1]); SB();
  const unsigned m1840 = pk(m1839[0], m1839[1]); SB();
  WAITC(63, 2);
  const f32x2 m1841 = PFMA(LO2(cs1804), n211, LO2(bb1811)); SB();
  const f32x2 m1853 = PFMA(HI2(cs1804), n211, HI2(bb1811)); SB();
  const f32x2 m1843 = PFMA(LO2(a11_1), r211, m1841); SB();
  const f32x2 m1855 = PFMA(HI2(a11_1), r211, m1853); SB();
  const float m1844 = __builtin_amdgcn_exp2f(m1843[0]); SB();
  const float m1845 = __builtin_amdgcn_exp2f(m1843[1]); SB();
  const float m1856 = __builtin_amdgcn_exp2f(m1855[0]); SB();
  const float m1857 = __builtin_amdgcn_exp2f(m1855[1]); SB();
  const f32x4 cs1806 = LDSF4(cbp + 384);
  const f32x4 bb1813 = LDSF4(cbp + 448);
  const f32x2 m1846 = kone + f32x2{m1844, m1845}; SB();
  const f32x2 m1858 = kone + f32x2{m1856, m1857}; SB();
  const f32x2 m1847 = PFMA(m1846, m1846, kone); SB();
  const f32x2 m1859 = PFMA(m1858, m1858, kone); SB();
  const float m1848 = __builtin_amdgcn_rcpf(m1847[0]); SB();
  const float m1849 = __builtin_amdgcn_rcpf(m1847[1]); SB();
  const float m1860 = __builtin_amdgcn_rcpf(m1859[0]); SB();
  const float m1861 = __builtin_amdgcn_rcpf(m1859[1]); SB();
  const f32x2 m1850 = PFMA((f32x2{m1848, m1849}), km2, kln2); SB();
  const f32x2 m1862 = PFMA((f32x2{m1860, m1861}), km2, kln2); SB();
  const f32x2 m1851 = m1843 * m1850; SB();
  const f32x2 m1863 = m1855 * m1862; SB();
  const unsigned m1852 = pk(m1851[0], m1851[1]); SB();
  const unsigned m1864 = pk(m1863[0], m1863[1]); SB();
  st16(v4u{m1828, m1840, m1852, m1864}, ws, vo0 + 0, hs11);
  WAITC(63, 2);
  const f32x2 m1865 = PFMA(LO2(cs1805), n211, LO2(bb1812)); SB();
  const f32x2 m1877 = PFMA(HI2(cs1805), n211, HI2(bb1812)); SB();
  const f32x2 m1867 = PFMA(LO2(a11_2), r211, m1865); SB();
  const f32x2 m1879 = PFMA(HI2(a11_2), r211, m1877); SB();
  const float m1868 = __builtin_amdgcn_exp2f(m1867[0]); SB();
  const float m1869 = __builtin_amdgcn_exp2f(m1867[1]); SB();
  const float m1880 = __builtin_amdgcn_exp2f(m1879[0]); SB();
  const float m1881 = __builtin_amdgcn_exp2f(m1879[1]); SB();
  const f32x4 cs1807 = LDSF4(cbp + 512);
  const f32x4 bb1814 = LDSF4(cbp + 576);
  const f32x2 m1870 = kone + f32x2{m1868, m1869}; SB();
  const f32x2 m1882 = kone + f32x2{m1880, m1881}; SB();
  const f32x2 m1871 = PFMA(m1870, m1870, kone); SB();
  const f32x2 m1883 = PFMA(m1882, m1882, kone); SB();
  const float m1872 = __builtin_amdgcn_rcpf(m1871[0]); SB();
  const float m1873 = __builtin_amdgcn_rcpf(m1871[1]); SB();
  const float m1884 = __builtin_amdgcn_rcpf(m1883[0]); SB();
  const float m1885 = __builtin_amdgcn_rcpf(m1883[1]); SB();
  const f32x2 m1874 = PFMA((f32x2{m1872, m1873}), km2, kln2); SB();
  const f32x2 m1886 = PFMA((f32x2{m1884, m1885}), km2, kln2); SB();
  const f32x2 m1875 = m1867 * m1874; SB();
  const f32x2 m1887 = m1879 * m1886; SB();
  const unsigned m1876 = pk(m1875[0], m1875[1]); SB();
  const unsigned m1888 = pk(m1887[0], m1887[1]); SB();
  WAITC(63, 2);
  const f32x2 m1889 = PFMA(LO2(cs1806), n211, LO2(bb1813)); SB();
  const f32x2 m1901 = PFMA(HI2(cs1806), n211, HI2(bb1813)); SB();
  const f32x2 m1891 = PFMA(LO2(a11_3), r211, m1889); SB();
  const f32x2 m1903 = PFMA(HI2(a11_3), r211, m1901); SB();
  const float m1892 = __builtin_amdgcn_exp2f(m1891[0]); SB();
  const float m1893 = __builtin_amdgcn_exp2f(m1891[1]); SB();
  const float m1904 = __builtin_amdgcn_exp2f(m1903[0]); SB();
  const float m1905 = __builtin_amdgcn_exp2f(m1903[1]); SB();
  const f32x4 cs1808 = LDSF4(cbp + 640);
  const f32x4 bb1815 = LDSF4(cbp + 704);
  const f32x2 m1894 = kone + f32x2{m1892, m1893}; SB();
  const f32x2 m1906 = kone + f32x2{m1904, m1905}; SB();
  const f32x2 m1895 = PFMA(m1894, m1894, kone); SB();
  const f32x2 m1907 = PFMA(m1906, m1906, kone); SB();
  const float m1896 = __builtin_amdgcn_rcpf(m1895[0]); SB();
  const float m1897 = __builtin_amdgcn_rcpf(m1895[1]); SB();
  const float m1908 = __builtin_amdgcn_rcpf(m1907[0]); SB();
  const float m1909 = __builtin_amdgcn_rcpf(m1907[1]); SB();
  const f32x2 m1898 = PFMA((f32x2{m1896, m1897}), km2, kln2); SB();
  const f32x2 m1910 = PFMA((f32x2{m1908, m1909}), km2, kln2); SB();
  const f32x2 m1899 = m1891 * m1898; SB();
  const f32x2 m1911 = m1903 * m1910; SB();
  const unsigned m1900 = pk(m1899[0], m1899[1]); SB();
  const unsigned m1912 = pk(m1911[0], m1911[1]); SB();
  st16(v4u{m1876, m1888, m1900, m1912}, ws, vo0 + 1024, hs11);
  WAITC(63, 2);
  const f32x2 m1913 = PFMA(LO2(cs1807), n211, LO2(bb1814)); SB();
  const f32x2 m1925 = PFMA(HI2(cs1807), n211, HI2(bb1814)); SB();
  const f32x2 m1915 = PFMA(LO2(a11_4), r211, m1913); SB();
  const f32x2 m1927 = PFMA(HI2(a11_4), r211, m1925); SB();
  const float m1916 = __builtin_amdgcn_exp2f(m1915[0]); SB();
  const float m1917 = __builtin_amdgcn_exp2f(m1915[1]); SB();
  const float m1928 = __builtin_amdgcn_exp2f(m1927[0]); SB();
  const float m1929 = __builtin_amdgcn_exp2f(m1927[1]); SB();
  const f32x4 cs1809 = LDSF4(cbp + 768);
  const f32x4 bb1816 = LDSF4(cbp + 832);
  const f32x2 m1918 = kone + f32x2{m1916, m1917}; SB();
  const f32x2 m1930 = kone + f32x2{m1928, m1929}; SB();
  const f32x2 m1919 = PFMA(m1918, m1918, kone); SB();
  const f32x2 m1931 = PFMA(m1930, m1930, kone); SB();
  const float m1920 = __builtin_amdgcn_rcpf(m1919[0]); SB();
  const float m1921 = __builtin_amdgcn_rcpf(m1919[1]); SB();
  const float m1932 = __builtin_amdgcn_rcpf(m1931[0]); SB();
  const float m1933 = __builtin_amdgcn_rcpf(m1931[1]); SB();
  const f32x2 m1922 = PFMA((f32x2{m1920, m1921}), km2, kln2); SB();
  const f32x2 m1934 = PFMA((f32x2{m1932, m1933}), km2, kln2); SB();
  const f32x2 m1923 = m1915 * m1922; SB();
  const f32x2 m1935 = m1927 * m1934; SB();
  const unsigned m1924 = pk(m1923[0], m1923[1]); SB();
  const unsigned m1936 = pk(m1935[0], m1935[1]); SB();
  WAITC(63, 2);
  const f32x2 m1937 = PFMA(LO2(cs1808), n211, LO2(bb1815)); SB();
  const f32x2 m1949 = PFMA(HI2(cs1808), n211, HI2(bb1815)); SB();
  const f32x2 m1939 = PFMA(LO2(a11_5), r211, m1937); SB();
  const f32x2 m1951 = PFMA(HI2(a11_5), r211, m1949); SB();
  const float m1940 = __builtin_amdgcn_exp2f(m1939[0]); SB();
  const float m1941 = __builtin_amdgcn_exp2f(m1939[1]); SB();
  const float m1952 = __builtin_amdgcn_exp2f(m1951[0]); SB();
  const float m1953 = __builtin_amdgcn_exp2f(m1951[1]); SB();
  const f32x2 m1942 = kone + f32x2{m1940, m1941}; SB();
  const f32x2 m1954 = kone + f32x2{m1952, m1953}; SB();
  const f32x2 m1943 = PFMA(m1942, m1942, kone); SB();
  const f32x2 m1955 = PFMA(m1954, m1954, kone); SB();
  const float m1944 = __builtin_amdgcn_rcpf(m1943[0]); SB();
  const float m1945 = __builtin_amdgcn_rcpf(m1943[1]); SB();
  const float m1956 = __builtin_amdgcn_rcpf(m1955[0]); SB();
  const float m1957 = __builtin_amdgcn_rcpf(m1955[1]); SB();
  const f32x2 m1946 = PFMA((f32x2{m1944, m1945}), km2, kln2); SB();
  const f32x2 m1958 = PFMA((f32x2{m1956, m1957}), km2, kln2); SB();
  const f32x2 m1947 = m1939 * m1946; SB();
  const f32x2 m1959 = m1951 * m1958; SB();
  const unsigned m1948 = pk(m1947[0], m1947[1]); SB();
  const unsigned m1960 = pk(m1959[0], m1959[1]); SB();
  st16(v4u{m1924, m1936, m1948, m1960}, ws, vo0 + 2048, hs11);
  WAITC(63, 0);
  const f32x2 m1961 = PFMA(LO2(cs1809), n211, LO2(bb1816)); SB();
  const f32x2 m1973 = PFMA(HI2(cs1809), n211, HI2(bb1816)); SB();
  const f32x2 m1963 = PFMA(LO2(a11_6), r211, m1961); SB();
  const f32x2 m1975 = PFMA(HI2(a11_6), r211, m1973); SB();
  const float m1964 = __builtin_amdgcn_exp2f(m1963[0]); SB();
  const float m1965 = __builtin_amdgcn_exp2f(m1963[1]); SB();
  const float m1976 = __builtin_amdgcn_exp2f(m1975[0]); SB();
  const float m1977 = __builtin_amdgcn_exp2f(m1975[1]); SB();
  const f32x2 m1966 = kone + f32x2{m1964, m1965}; SB();
  const f32x2 m1978 = kone + f32x2{m1976, m1977}; SB();
  const f32x2 m1967 = PFMA(m1966, m1966, kone); SB();
  const f32x2 m1979 = PFMA(m1978, m1978, kone); SB();
  const float m1968 = __builtin_amdgcn_rcpf(m1967[0]); SB();
  const float m1969 = __builtin_amdgcn_rcpf(m1967[1]); SB();
  const float m1980 = __builtin_amdgcn_rcpf(m1979[0]); SB();
  const float m1981 = __builtin_amdgcn_rcpf(m1979[1]); SB();
  const f32x2 m1970 = PFMA((f32x2{m1968, m1969}), km2, kln2); SB();
  const f32x2 m1982 = PFMA((f32x2{m1980, m1981}), km2, kln2); SB();
  const f32x2 m1971 = m1963 * m1970; SB();
  const f32x2 m1983 = m1975 * m1982; SB();
  const unsigned m1972 = pk(m1971[0], m1971[1]); SB();
  const unsigned m1984 = pk(m1983[0], m1983[1]); SB();
  st8(v2u{m1972, m1984}, ws, vl8, ht11);
  STAMP(23);
  WAITC(14, 15);
  f32x4 c100_0; MFA0(c100_0, W0, h100_0);
  int so1646; SADDS(so1646, hb100, ko11);
  v4u h100_11 = ld16(ws, vo0 + 0, so1646, SC1);
  int so1647; SADD(so1647, hb100, 3072);
  f32x4 c100_1; MFA0(c100_1, W1, h100_0);
  f32x4 c100_2; MFA0(c100_2, W2, h100_0);
  f32x4 c100_3; MFA0(c100_3, W3, h100_0);
  f32x4 c100_4; MFA0(c100_4, W4, h100_0);
  WAITC(14, 15);
  const v2u h100_12a = ld8(ws, vha, so1647, SC1);
  MFA(c100_0, W5, h100_1);
  MFA(c100_1, W6, h100_1);
  MFA(c100_2, W7, h100_1);
  MFA(c100_3, W8, h100_1);
  const v2u h100_12b = ld8(ws, vhb, so1647, SC1);
  int so1648; SADD(so1648, hb100, 10240);
  MFA(c100_4, W9, h100_1);
  WAITC(15, 15);
  MFA(c100_0, W10, h100_2);
  MFA(c100_1, W11, h100_2);
  MFA(c100_2, W12, h100_2);
  const v2u h100_13a = ld8(ws, vha, so1648, SC1);
  MFA(c100_3, W13, h100_2);
  MFA(c100_4, W14, h100_2);
  WAITC(15, 15);
  MFA(c100_0, W15, h100_3);
  MFA(c100_1, W16, h100_3);
  const v2u h100_13b = ld8(ws, vhb, so1648, SC1);
  int so1649; SADDS(so1649, hb101, ko0);
  MFA(c100_2, W17, h100_3);
  MFA(c100_3, W18, h100_3);
  MFA(c100_4, W19, h100_3);
  WAITC(15, 15);
  MFA(c100_0, W20, h100_4);
  v4u h101_0 = ld16(ws, vo0 + 0, so1649, SC1);
  int so1650; SADDS(so1650, hb101, ko1);
  MFA(c100_1, W21, h100_4);
  MFA(c100_2, W22, h100_4);
  MFA(c100_3, W23, h100_4);
  MFA(c100_4, W24, h100_4);
  WAITC(15, 15);
  v4u h101_1 = ld16(ws, vo0 + 0, so1650, SC1);
  int so1651; SADDS(so1651, hb101, ko2);
  MFA(c100_0, W25, h100_5);
  MFA(c100_1, W26, h100_5);
  MFA(c100_2, W27, h100_5);
  MFA(c100_3, W28, h100_5);
  v4u h101_2 = ld16(ws, vo0 + 0, so1651, SC1);
  int so1652; SADDS(so1652, hb101, ko3);
  MFA(c100_4, W29, h100_5);
  WAITC(16, 15);
  MFA(c100_0, W30, h100_6);
  MFA(c100_1, W31, h100_6);
  MFA(c100_2, W32, h100_6);
  v4u h101_3 = ld16(ws, vo0 + 0, so1652, SC1);
  int so1653; SADDS(so1653, hb101, ko4);
  MFA(c100_3, W33, h100_6);
  MFA(c100_4, W34, h100_6);
  WAITC(16, 15);
  MFA(c100_0, W35, h100_7);
  MFA(c100_1, W36, h100_7);
  v4u h101_4 = ld16(ws, vo0 + 0, so1653, SC1);
  MFA(c100_2, W37, h100_7);
  MFA(c100_3, W38, h100_7);
  MFA(c100_4, W39, h100_7);
  WAITC(16, 15);
  MFA(c100_0, W40, h100_8);
  DRAINBAR(10);
  MFA(c100_1, W41, h100_8);
  ARRIVE(va1, (gl + 5) * 256);
  MFA(c100_2, W42, h100_8);
  int so1654; SADDS(so1654, hb101, ko5);
  MFA(c100_3, W43, h100_8);
  MFA(c100_4, W44, h100_8);
  MFA(c100_0, W45, h100_9);
  v4u h101_5 = ld16(ws, vo0 + 0, so1654, SC1);
  int so1655; SADDS(so1655, hb101, ko6);
  MFA(c100_1, W46, h100_9);
  MFA(c100_2, W47, h100_9);
  MFA(c100_3, W48, h100_9);
  MFA(c100_4, W49, h100_9);
  v4u h101_6 = ld16(ws, vo0 + 0, so1655, SC1);
  int so1656; SADDS(so1656, hb101, ko7);
  MFA(c100_0, W50, h100_10);
  MFA(c100_1, W51, h100_10);
  MFA(c100_2, W52, h100_10);
  MFA(c100_3, W53, h100_10);
  v4u h101_7 = ld16(ws, vo0 + 0, so1656, SC1);
  int so1657; SADDS(so1657, hb101, ko8);
  MFA(c100_4, W54, h100_10);
  WAITC(13, 15);
  MFA(c100_0, W55, h100_11);
  MFA(c100_1, W56, h100_11);
  MFA(c100_2, W57, h100_11);
  v4u h101_8 = ld16(ws, vo0 + 0, so1657, SC1);
  int so1658; SADDS(so1658, hb101, ko9);
  MFA(c100_3, W58, h100_11);
  MFA(c100_4, W59, h100_11);
  WAITC(12, 15);
  MFA(c100_0, W60, cat(h100_12a, h100_12b));
  MFA(c100_1, W61, cat(h100_12a, h100_12b));
  v4u h101_9 = ld16(ws, vo0 + 0, so1658, SC1);
  int so1659; SADDS(so1659, hb101, ko10);
  MFA(c100_2, W62, cat(h100_12a, h100_12b));
  MFA(c100_3, W63, cat(h100_12a, h100_12b));
  MFV(c100_4, W64, cat(h100_12a, h100_12b));
  WAITC(11, 15);
  MFV(c100_0, W65, cat(h100_13a, h100_13b));
  v4u h101_10 = ld16(ws, vo0 + 0, so1659, SC1);
  int so1660; SADDS(so1660, hb101, ko11);
  MFV(c100_1, W66, cat(h100_13a, h100_13b));
  MFV(c100_2, W67, cat(h100_13a, h100_13b));
  MFV(c100_3, W68, cat(h100_13a, h100_13b));
  MFV(c100_4, W69, cat(h100_13a, h100_13b));
  WAITC(11, 15);
  v4u h101_11 = ld16(ws, vo0 + 0, so1660, SC1);
  int so1661; SADD(so1661, hb101, 3072);
  f32x4 c101_2; MFA0(c101_2, W2, h101_0);
  f32x4 c101_3; MFA0(c101_3, W3, h101_0);
  f32x4 c101_4; MFA0(c101_4, W4, h101_0);
  WAITC(11, 15);
  MFA(c101_2, W7, h101_1);
  const v2u h101_12a = ld8(ws, vha, so1661, SC1);
  MFA(c101_3, W8, h101_1);
  LDSF4(xw0 + 0) = c100_0;
  MFA(c101_4, W9, h101_1);
  WAITC(11, 15);
  MFA(c101_2, W12, h101_2);
  MFA(c101_3, W13, h101_2);
  const v2u h101_12b = ld8(ws, vhb, so1661, SC1);
  int so1662; SADD(so1662, hb101, 10240);
  MFA(c101_4, W14, h101_2);
  WAITC(11, 15);
  LDSF4(xw0 + 1024) = c100_1;
  MFA(c101_2, W17, h101_3);
  MFA(c101_3, W18, h101_3);
  MFA(c101_4, W19, h101_3);
  WAITC(10, 15);
  POLL_LD(pl1985, vp1, (gl2 + 5) * 256);
  MFA(c101_2, W22, h101_4);
  LDSF4(xw2 + 2048) = c100_2;
  MFA(c101_3, W23, h101_4);
  MFA(c101_4, W24, h101_4);
  WAITC(9, 15);
  MFA(c101_2, W27, h101_5);
  const v2u h101_13a = ld8(ws, vha, so1662, SC1);
  MFA(c101_3, W28, h101_5);
  LDSF4(xw2 + 3072) = c100_3;
  MFA(c101_4, W29, h101_5);
  WAITC(9, 15);
  MFA(c101_2, W32, h101_6);
  MFA(c101_3, W33, h101_6);
  const v2u h101_13b = ld8(ws, vhb, so1662, SC1);
  MFA(c101_4, W34, h101_6);
  WAITC(9, 15);
  LDSF4(xw2 + 4096) = c100_4;
  MFA(c101_2, W37, h101_7);
  MFA(c101_3, W38, h101_7);
  MFA(c101_4, W39, h101_7);
  WAITC(8, 15);
  MFA(c101_2, W42, h101_8);
  MFA(c101_3, W43, h101_8);
  WAITC(63, 0);
  BARRIER();
  const f32x4 xa2019 = LDSF4(xr0 + 0);
  MFA(c101_4, W44, h101_8);
  WAITC(7, 15);
  const f32x4 xb2020 = LDSF4(xr1 + 0);
  const f32x4 xa2021 = LDSF4(xr0 + 1024);
  MFA(c101_2, W47, h101_9);
  const f32x4 xb2022 = LDSF4(xr1 + 1024);
  const f32x4 xa2023 = LDSF4(xr0 + 2048);
  MFA(c101_3, W48, h101_9);
  const f32x4 xb2024 = LDSF4(xr1 + 2048);
  MFA(c101_4, W49, h101_9);
  WAITC(6, 15);
  MFA(c101_2, W52, h101_10);
  MFA(c101_3, W53, h101_10);
  WAITC(63, 4);
  float z2025; OP2("v_add_f32", z2025, xa2019[0], xb2020[0]);
  float z2026; OP2("v_add_f32", z2026, xa2019[1], xb2020[1]);
  MFA(c101_4, W54, h101_10);
  WAITC(5, 15);
  float z2027; OP2("v_add_f32", z2027, xa2019[2], xb2020[2]);
  float z2028; OP2("v_add_f32", z2028, xa2019[3], xb2020[3]);
  MFA(c101_2, W57, h101_11);
  WAITC(63, 2);
  float z2029; OP2("v_add_f32", z2029, xa2021[0], xb2022[0]);
  float z2030; OP2("v_add_f32", z2030, xa2021[1], xb2022[1]);
  MFA(c101_3, W58, h101_11);
  float z2031; OP2("v_add_f32", z2031, xa2021[2], xb2022[2]);
  float z2032; OP2("v_add_f32", z2032, xa2021[3], xb2022[3]);
  MFA(c101_4, W59, h101_11);
  WAITC(3, 0);
  float z2033; OP2("v_add_f32", z2033, xa2023[0], xb2024[0]);
  float z2034; OP2("v_add_f32", z2034, xa2023[1], xb2024[1]);
  MFA(c101_2, W62, cat(h101_12a, h101_12b));
  float z2035; OP2("v_add_f32", z2035, xa2023[2], xb2024[2]);
  float z2036; OP2("v_add_f32", z2036, xa2023[3], xb2024[3]);
  MFA(c101_3, W63, cat(h101_12a, h101_12b));
  MFV(c101_4, W64, cat(h101_12a, h101_12b));
  WAITC(0, 15);
  POLL_CK(pl1985, vp1, (gl2 + 5) * 256, 16);
  MFV(c101_2, W67, cat(h101_13a, h101_13b));
  int so1986; SADDS(so1986, hb110, ko0);
  v4u h110_0 = ld16(ws, vo0 + 0, so1986, SC1);
  int so1987; SADDS(so1987, hb110, ko1);
  MFV(c101_3, W68, cat(h101_13a, h101_13b));
  MFV(c101_4, W69, cat(h101_13a, h101_13b));
  f32x4 c101_0; MFA0(c101_0, W0, h101_0);
  f32x4 c101_1; MFA0(c101_1, W1, h101_0);
  v4u h110_1 = ld16(ws, vo0 + 0, so1987, SC1);
  int so1988; SADDS(so1988, hb110, ko2);
  MFA(c101_0, W5, h101_1);
  MFA(c101_1, W6, h101_1);
  MFA(c101_0, W10, h101_2);
  LDSF4(xr1) = c101_2;
  MFA(c101_1, W11, h101_2);
  v4u h110_2 = ld16(ws, vo0 + 0, so1988, SC1);
  int so1989; SADDS(so1989, hb110, ko3);
  MFA(c101_0, W15, h101_3);
  MFA(c101_1, W16, h101_3);
  MFA(c101_0, W20, h101_4);
  LDSF4(xr0) = c101_3;
  MFA(c101_1, W21, h101_4);
  v4u h110_3 = ld16(ws, vo0 + 0, so1989, SC1);
  int so1990; SADDS(so1990, hb110, ko4);
  MFA(c101_0, W25, h101_5);
  MFA(c101_1, W26, h101_5);
  MFA(c101_0, W30, h101_6);
  LDSF4(xr0 + 1024) = c101_4;
  MFA(c101_1, W31, h101_6);
  float z2040; OP2("v_add_f32", z2040, z2033, c101_2[0]);
  float z2041; OP2("v_add_f32", z2041, z2034, c101_2[1]);
  MFA(c101_0, W35, h101_7);
  float z2042; OP2("v_add_f32", z2042, z2035, c101_2[2]);
  float z2043; OP2("v_add_f32", z2043, z2036, c101_2[3]);
  MFA(c101_1, W36, h101_7);
  v4u h110_4 = ld16(ws, vo0 + 0, so1990, SC1);
  int so1991; SADDS(so1991, hb110, ko5);
  MFA(c101_0, W40, h101_8);
  WAITC(63, 0);

  BARRIER();
  const f32x4 ya2044 = LDSF4(yq0);
  MFA(c101_1, W41, h101_8);
  const f32x4 ya2045 = LDSF4(yq1);
  const f32x4 ya2046 = LDSF4(yr2a + 1024);
  MFA(c101_0, W45, h101_9);
  MFA(c101_1, W46, h101_9);
  v4u h110_5 = ld16(ws, vo0 + 0, so1991, SC1);
  int so1992; SADDS(so1992, hb110, ko6);
  MFA(c101_0, W50, h101_10);
  MFA(c101_1, W51, h101_10);
  MFA(c101_0, W55, h101_11);
  MFA(c101_1, W56, h101_11);
  v4u h110_6 = ld16(ws, vo0 + 0, so1992, SC1);
  MFA(c101_0, W60, cat(h101_12a, h101_12b));
  MFA(c101_1, W61, cat(h101_12a, h101_12b));
  MFV(c101_0, W65, cat(h101_13a, h101_13b));
  MFV(c101_1, W66, cat(h101_13a, h101_13b));
  WAITC(63, 0);
  STAMP(24);
  NOP8(); TIE(c101_0); TIE(c101_1);
  f32x4 r10_0 = (f32x4{z2025, z2026, z2027, z2028} + c101_0) + ya2044;
  f32x4 r10_1 = (f32x4{z2029, z2030, z2031, z2032} + c101_1) + ya2045;
  f32x4 r10_2 = (f32x4{z2040, z2041, z2042, z2043}) + ya2046;
  int so1993; SADDS(so1993, hb110, ko7);
  v4u h110_7 = ld16(ws, vo0 + 0, so1993, SC1);
  int so1994; SADDS(so1994, hb110, ko8);
  v4u h110_8 = ld16(ws, vo0 + 0, so1994, SC1);
  EPI_OUT(0, 0);
  int so1995; SADDS(so1995, hb110, ko9);
  v4u h110_9 = ld16(ws, vo0 + 0, so1995, SC1);
  int so1996; SADDS(so1996, hb110, ko10);
  v4u h110_10 = ld16(ws, vo0 + 0, so1996, SC1);
  int so1997; SADDS(so1997, hb110, ko11);
  v4u h110_11 = ld16(ws, vo0 + 0, so1997, SC1);
  STAMP(25);
  WAITC(13, 15);
  f32x4 c110_0; MFA0(c110_0, W0, h110_0);
  int so1998; SADD(so1998, hb110, 3072);
  const v2u h110_12a = ld8(ws, vha, so1998, SC1);
  f32x4 c110_1; MFA0(c110_1, W1, h110_0);
  f32x4 c110_2; MFA0(c110_2, W2, h110_0);
  f32x4 c110_3; MFA0(c110_3, W3, h110_0);
  f32x4 c110_4; MFA0(c110_4, W4, h110_0);
  WAITC(13, 15);
  const v2u h110_12b = ld8(ws, vhb, so1998, SC1);
  int so1999; SADD(so1999, hb110, 10240);
  MFA(c110_0, W5, h110_1);
  MFA(c110_1, W6, h110_1);
  MFA(c110_2, W7, h110_1);
  MFA(c110_3, W8, h110_1);
  const v2u h110_13a = ld8(ws, vha, so1999, SC1);
  MFA(c110_4, W9, h110_1);
  WAITC(14, 15);
  MFA(c110_0, W10, h110_2);
  MFA(c110_1, W11, h110_2);
  MFA(c110_2, W12, h110_2);
  const v2u h110_13b = ld8(ws, vhb, so1999, SC1);
  int so2000; SADDS(so2000, hb111, ko0);
  MFA(c110_3, W13, h110_2);
  MFA(c110_4, W14, h110_2);
  WAITC(14, 15);
  MFA(c110_0, W15, h110_3);
  MFA(c110_1, W16, h110_3);
  v4u h111_0 = ld16(ws, vo0 + 0, so2000, SC1);
  int so2001; SADDS(so2001, hb111, ko1);
  MFA(c110_2, W17, h110_3);
  MFA(c110_3, W18, h110_3);
  MFA(c110_4, W19, h110_3);
  WAITC(14, 15);
  MFA(c110_0, W20, h110_4);

  v4u h111_1 = ld16(ws, vo0 + 0, so2001, SC1);
  int so2002; SADDS(so2002, hb111, ko2);
  MFA(c110_1, W21, h110_4);
  MFA(c110_2, W22, h110_4);
  MFA(c110_3, W23, h110_4);
  MFA(c110_4, W24, h110_4);
  WAITC(14, 15);
  v4u h111_2 = ld16(ws, vo0 + 0, so2002, SC1);
  int so2003; SADDS(so2003, hb111, ko3);
  MFA(c110_0, W25, h110_5);
  MFA(c110_1, W26, h110_5);
  MFA(c110_2, W27, h110_5);
  MFA(c110_3, W28, h110_5);
  v4u h111_3 = ld16(ws, vo0 + 0, so2003, SC1);
  int so2004; SADDS(so2004, hb111, ko4);
  MFA(c110_4, W29, h110_5);
  WAITC(15, 15);
  MFA(c110_0, W30, h110_6);
  MFA(c110_1, W31, h110_6);
  MFA(c110_2, W32, h110_6);
  v4u h111_4 = ld16(ws, vo0 + 0, so2004, SC1);
  int so2005; SADDS(so2005, hb111, ko5);
  MFA(c110_3, W33, h110_6);
  MFA(c110_4, W34, h110_6);
  WAITC(15, 15);
  MFA(c110_0, W35, h110_7);
  MFA(c110_1, W36, h110_7);
  v4u h111_5 = ld16(ws, vo0 + 0, so2005, SC1);
  int so2006; SADDS(so2006, hb111, ko6);
  MFA(c110_2, W37, h110_7);
  MFA(c110_3, W38, h110_7);
  MFA(c110_4, W39, h110_7);
  WAITC(15, 15);
  MFA(c110_0, W40, h110_8);
  v4u h111_6 = ld16(ws, vo0 + 0, so2006, SC1);
  int so2007; SADDS(so2007, hb111, ko7);
  MFA(c110_1, W41, h110_8);
  MFA(c110_2, W42, h110_8);
  MFA(c110_3, W43, h110_8);
  MFA(c110_4, W44, h110_8);
  WAITC(13, 15);
  v4u h111_7 = ld16(ws, vo0 + 0, so2007, SC1);
  int so2008; SADDS(so2008, hb111, ko8);
  MFA(c110_0, W45, h110_9);
  MFA(c110_1, W46, h110_9);
  MFA(c110_2, W47, h110_9);
  MFA(c110_3, W48, h110_9);
  v4u h111_8 = ld16(ws, vo0 + 0, so2008, SC1);
  int so2009; SADDS(so2009, hb111, ko9);
  MFA(c110_4, W49, h110_9);
  WAITC(14, 15);
  MFA(c110_0, W50, h110_10);
  MFA(c110_1, W51, h110_10);
  MFA(c110_2, W52, h110_10);
  v4u h111_9 = ld16(ws, vo0 + 0, so2009, SC1);
  int so2010; SADDS(so2010, hb111, ko10);
  MFA(c110_3, W53, h110_10);
  MFA(c110_4, W54, h110_10);
  WAITC(14, 15);
  MFA(c110_0, W55, h110_11);
  MFA(c110_1, W56, h110_11);
  v4u h111_10 = ld16(ws, vo0 + 0, so2010, SC1);
  int so2011; SADDS(so2011, hb111, ko11);
  MFA(c110_2, W57, h110_11);
  MFA(c110_3, W58, h110_11);
  MFA(c110_4, W59, h110_11);
  WAITC(13, 15);
  MFA(c110_0, W60, cat(h110_12a, h110_12b));
  BARRIER();
  v4u h111_11 = ld16(ws, vo0 + 0, so2011, SC1);
  MFA(c110_1, W61, cat(h110_12a, h110_12b));
  int so2012; SADD(so2012, hb111, 3072);
  MFA(c110_2, W62, cat(h110_12a, h110_12b));
  MFA(c110_3, W63, cat(h110_12a, h110_12b));
  MFV(c110_4, W64, cat(h110_12a, h110_12b));
  WAITC(12, 15);
  const v2u h111_12a = ld8(ws, vha, so2012, SC1);
  MFV(c110_0, W65, cat(h110_13a, h110_13b));
  MFV(c110_1, W66, cat(h110_13a, h110_13b));
  MFV(c110_2, W67, cat(h110_13a, h110_13b));
  MFV(c110_3, W68, cat(h110_13a, h110_13b));
  const v2u h111_12b = ld8(ws, vhb, so2012, SC1);
  int so2013; SADD(so2013, hb111, 10240);
  MFV(c110_4, W69, cat(h110_13a, h110_13b));
  WAITC(13, 15);
  f32x4 c111_2; MFA0(c111_2, W2, h111_0);
  f32x4 c111_3; MFA0(c111_3, W3, h111_0);
  f32x4 c111_4; MFA0(c111_4, W4, h111_0);
  WAITC(12, 15);
  const v2u h111_13a = ld8(ws, vha, so2013, SC1);
  MFA(c111_2, W7, h111_1);
  MFA(c111_3, W8, h111_1);
  LDSF4(xw0 + 0) = c110_0;
  MFA(c111_4, W9, h111_1);
  WAITC(12, 15);
  MFA(c111_2, W12, h111_2);
  const v2u h111_13b = ld8(ws, vhb, so2013, SC1);
  MFA(c111_3, W13, h111_2);
  MFA(c111_4, W14, h111_2);
  WAITC(12, 15);
  LDSF4(xw0 + 1024) = c110_1;
  MFA(c111_2, W17, h111_3);
  MFA(c111_3, W18, h111_3);
  MFA(c111_4, W19, h111_3);
  WAITC(11, 15);
  MFA(c111_2, W22, h111_4);
  LDSF4(xw2 + 2048) = c110_2;
  MFA(c111_3, W23, h111_4);
  MFA(c111_4, W24, h111_4);
  WAITC(10, 15);
  MFA(c111_2, W27, h111_5);
  MFA(c111_3, W28, h111_5);
  LDSF4(xw2 + 3072) = c110_3;
  MFA(c111_4, W29, h111_5);
  WAITC(9, 15);
  MFA(c111_2, W32, h111_6);
  MFA(c111_3, W33, h111_6);
  MFA(c111_4, W34, h111_6);
  WAITC(8, 15);
  LDSF4(xw2 + 4096) = c110_4;
  MFA(c111_2, W37, h111_7);
  MFA(c111_3, W38, h111_7);
  MFA(c111_4, W39, h111_7);
  WAITC(7, 15);
  MFA(c111_2, W42, h111_8);
  MFA(c111_3, W43, h111_8);
  WAITC(63, 0);
  BARRIER();
  const f32x4 xa2052 = LDSF4(xr0 + 0);
  MFA(c111_4, W44, h111_8);
  WAITC(6, 15);
  const f32x4 xb2053 = LDSF4(xr1 + 0);
  const f32x4 xa2054 = LDSF4(xr0 + 1024);
  MFA(c111_2, W47, h111_9);
  const f32x4 xb2055 = LDSF4(xr1 + 1024);
  const f32x4 xa2056 = LDSF4(xr0 + 2048);
  MFA(c111_3, W48, h111_9);
  const f32x4 xb2057 = LDSF4(xr1 + 2048);
  MFA(c111_4, W49, h111_9);
  WAITC(5, 15);
  MFA(c111_2, W52, h111_10);
  MFA(c111_3, W53, h111_10);
  WAITC(63, 4);
  float z2058; OP2("v_add_f32", z2058, xa2052[0], xb2053[0]);
  float z2059; OP2("v_add_f32", z2059, xa2052[1], xb2053[1]);
  MFA(c111_4, W54, h111_10);
  WAITC(4, 15);
  float z2060; OP2("v_add_f32", z2060, xa2052[2], xb2053[2]);
  float z2061; OP2("v_add_f32", z2061, xa2052[3], xb2053[3]);
  MFA(c111_2, W57, h111_11);
  WAITC(63, 2);
  float z2062; OP2("v_add_f32", z2062, xa2054[0], xb2055[0]);
  float z2063; OP2("v_add_f32", z2063, xa2054[1], xb2055[1]);
  MFA(c111_3, W58, h111_11);
  float z2064; OP2("v_add_f32", z2064, xa2054[2], xb2055[2]);
  float z2065; OP2("v_add_f32", z2065, xa2054[3], xb2055[3]);
  MFA(c111_4, W59, h111_11);
  WAITC(2, 0);
  float z2066; OP2("v_add_f32", z2066, xa2056[0], xb2057[0]);
  float z2067; OP2("v_add_f32", z2067, xa2056[1], xb2057[1]);
  MFA(c111_2, W62, cat(h111_12a, h111_12b));
  float z2068; OP2("v_add_f32", z2068, xa2056[2], xb2057[2]);
  float z2069; OP2("v_add_f32", z2069, xa2056[3], xb2057[3]);
  MFA(c111_3, W63, cat(h111_12a, h111_12b));
  MFV(c111_4, W64, cat(h111_12a, h111_12b));
  WAITC(0, 15);
  MFV(c111_2, W67, cat(h111_13a, h111_13b));
  MFV(c111_3, W68, cat(h111_13a, h111_13b));
  MFV(c111_4, W69, cat(h111_13a, h111_13b));
  f32x4 c111_0; MFA0(c111_0, W0, h111_0);
  f32x4 c111_1; MFA0(c111_1, W1, h111_0);
  MFA(c111_0, W5, h111_1);
  MFA(c111_1, W6, h111_1);
  MFA(c111_0, W10, h111_2);
  LDSF4(xr1) = c111_2;
  MFA(c111_1, W11, h111_2);
  MFA(c111_0, W15, h111_3);
  MFA(c111_1, W16, h111_3);
  MFA(c111_0, W20, h111_4);
  LDSF4(xr0) = c111_3;
  MFA(c111_1, W21, h111_4);
  MFA(c111_0, W25, h111_5);
  MFA(c111_1, W26, h111_5);
  MFA(c111_0, W30, h111_6);
  LDSF4(xr0 + 1024) = c111_4;
  MFA(c111_1, W31, h111_6);
  float z2073; OP2("v_add_f32", z2073, z2066, c111_2[0]);
  float z2074; OP2("v_add_f32", z2074, z2067, c111_2[1]);
  MFA(c111_0, W35, h111_7);
  float z2075; OP2("v_add_f32", z2075, z2068, c111_2[2]);
  float z2076; OP2("v_add_f32", z2076, z2069, c111_2[3]);
  MFA(c111_1, W36, h111_7);
  MFA(c111_0, W40, h111_8);
  WAITC(63, 0);

  BARRIER();
  const f32x4 ya2077 = LDSF4(yq0);
  MFA(c111_1, W41, h111_8);
  const f32x4 ya2078 = LDSF4(yq1);
  const f32x4 ya2079 = LDSF4(yr2a + 1024);
  MFA(c111_0, W45, h111_9);
  MFA(c111_1, W46, h111_9);
  MFA(c111_0, W50, h111_10);
  MFA(c111_1, W51, h111_10);
  MFA(c111_0, W55, h111_11);
  MFA(c111_1, W56, h111_11);
  MFA(c111_0, W60, cat(h111_12a, h111_12b));
  MFA(c111_1, W61, cat(h111_12a, h111_12b));
  MFV(c111_0, W65, cat(h111_13a, h111_13b));
  MFV(c111_1, W66, cat(h111_13a, h111_13b));
  WAITC(63, 0);
  STAMP(26);
  NOP8(); TIE(c111_0); TIE(c111_1);
  f32x4 r11_0 = (f32x4{z2058, z2059, z2060, z2061} + c111_0) + ya2077;
  f32x4 r11_1 = (f32x4{z2062, z2063, z2064, z2065} + c111_1) + ya2078;
  f32x4 r11_2 = (f32x4{z2073, z2074, z2075, z2076}) + ya2079;
  EPI_OUT(1, 0);
  STAMP(40);
}

extern "C" void kernel_launch(void* const* d_in, const int* in_sizes, int n_in, void* d_out, int out_size, void* d_ws, size_t ws_size, hipStream_t stream) {
  (void)in_sizes; (void)n_in; (void)out_size; (void)ws_size;
  (void)hipFuncSetAttribute((const void*)h3_mish_kernel, hipFuncAttributeMaxDynamicSharedMemorySize, LDS_BYTES);
  h3_mish_kernel<<<256, 256, LDS_BYTES, stream>>>((const char*)d_in[0], d_in[1], d_in[2], d_in[3], d_in[4], d_in[5], d_in[7], d_in[8], d_in[9], d_out, (char*)d_ws + 0);
}
```
